```python
import torch

def _softmax(x, axis=-1):
    return torch.softmax(x, dim=axis)
def _gelu(x):
    return torch.nn.functional.gelu(x, approximate="tanh")
def _silu(x):
    return torch.nn.functional.silu(x)
def _sigmoid(x):
    return torch.sigmoid(x)
def _layernorm(x, axis=-1, eps=1e-5):
    m = x.mean(dim=axis, keepdim=True)
    v = x.var(dim=axis, keepdim=True, unbiased=False)
    return (x - m) / torch.sqrt(v + eps)
def _scatter_add(tgt, idx, src, axis=0):
    return tgt.clone().index_add_(axis, idx, src)
def _segment_sum(src, seg, n):
    out = torch.zeros((n,) + src.shape[1:], dtype=src.dtype)
    return out.index_add_(0, seg, src)
def _pad(x, pw):
    flat = tuple(v for pair in reversed(pw) for v in pair)
    return torch.nn.functional.pad(x, flat)

kernel_name = 'gated_mix__ssm_conv_s4d__ssm_conv_h3__prompt'

def setup_inputs():

    _w = (lambda a: (lambda t: t.to(torch.bfloat16) if t.is_floating_point() else t)(torch.as_tensor(a)))
    def _A_setup():
        Bsz, L, D, N = 1, 2048, 1024, 128
        return {
            "u": _w((lambda *s: torch.randn(*s, dtype=torch.bfloat16) * 0.05)(Bsz, L, D)),
            "log_A_real": _w((lambda *s: torch.randn(*s, dtype=torch.bfloat16) * 3.2)(D, N) * 0.5),
            "A_imag": _w((lambda *s: torch.randn(*s, dtype=torch.bfloat16) * 0.05)(D, N) * 3.14),
            "C_real": _w((lambda *s: torch.randn(*s, dtype=torch.bfloat16) * 0.1)(D, N) / N ** 0.5),
            "C_imag": _w((lambda *s: torch.randn(*s, dtype=torch.bfloat16) * 0.1)(D, N) / N ** 0.5),
            "log_dt": _w((lambda *s: torch.randn(*s, dtype=torch.bfloat16) * 0.05)(D) * 0.5 - 4.0),
            "D_skip": _w((lambda *s: torch.randn(*s, dtype=torch.bfloat16) * 0.05)(D)),
        }
    _A_in = _A_setup()

    def _B_setup():
        B, S, D = 1, 2048, 1024
        ks = 4
        return {
            "q": _w((lambda *s: torch.randn(*s, dtype=torch.bfloat16) * 0.05)(B, S, D)),
            "k": _w((lambda *s: torch.randn(*s, dtype=torch.bfloat16) * 0.4)(B, S, D)),
            "v": _w((lambda *s: torch.randn(*s, dtype=torch.bfloat16) * 0.2)(B, S, D)),
            "shift_k": _w((lambda *s: torch.randn(*s, dtype=torch.bfloat16) * 0.4)(D, ks) * 0.5),
            "ssm_k": _w((lambda *s: torch.randn(*s, dtype=torch.bfloat16) * 0.4)(D, S) * (1.0 / S ** 0.5)),
            "ssm_D": _w((lambda *s: torch.randn(*s, dtype=torch.bfloat16) * 0.4)(D)),
        }
    _B_in = _B_setup()

    return {
        "A_u": _A_in["u"],
        "A_log_A_real": _A_in["log_A_real"],
        "A_A_imag": _A_in["A_imag"],
        "A_C_real": _A_in["C_real"],
        "A_C_imag": _A_in["C_imag"],
        "A_log_dt": _A_in["log_dt"],
        "A_D_skip": _A_in["D_skip"],
        "B_q": _B_in["q"],
        "B_k": _B_in["k"],
        "B_v": _B_in["v"],
        "B_shift_k": _B_in["shift_k"],
        "B_ssm_k": _B_in["ssm_k"],
        "B_ssm_D": _B_in["ssm_D"],
        "gate_w": _w((lambda *s: torch.randn(*s, dtype=torch.bfloat16) * 0.05)(1024) * 16.0),
    }

def reference(A_u, A_log_A_real, A_A_imag, A_C_real, A_C_imag, A_log_dt, A_D_skip, B_q, B_k, B_v, B_shift_k, B_ssm_k, B_ssm_D, gate_w):
    def _A_ref(u, log_A_real, A_imag, C_real, C_imag, log_dt, D_skip):
        L = u.shape[1]
        dt = torch.exp(log_dt)
        A = -torch.exp(log_A_real) + 1j * A_imag
        C = (C_real + 1j * C_imag) * dt[:, None]
        pos = (lambda x, dt: x.to(dt))(torch.arange(L), log_dt.dtype)
        P = torch.exp((A * dt[:, None])[:, :, None] * pos[None, None, :])
        K = 2.0 * torch.einsum("dn,dnl->dl", C, P).real
        L2 = 2 * L
        u_t = (lambda x, *d: x.permute(*d))(u, 0, 2, 1)
        Kf = (lambda x, *a, **k: torch.fft.rfft(x.float(), *a, **k))(K, L2)
        Uf = (lambda x, *a, **k: torch.fft.rfft(x.float(), *a, **k))(u_t, L2)
        y = (lambda x, *a, **k: torch.fft.irfft(x, *a, **k).to(torch.bfloat16))(Uf * Kf[None, :, :], L2)[:, :, :L]
        y = (lambda x, *d: x.permute(*d))(y, 0, 2, 1)
        return y + u * D_skip[None, None, :]

    def _B_ref(q, k, v, shift_k, ssm_k, ssm_D):
        B, S, D = q.shape
        ks = shift_k.shape[-1]
        k_pad = _pad(k, ((0, 0), (ks - 1, 0), (0, 0)))
        k_shift = torch.zeros_like(k)
        for t in range(ks):
            k_shift = k_shift + k_pad[:, t:t + S, :] * shift_k[:, ks - 1 - t][None, None, :]
        kv = k_shift * v
        L = 2 * S
        kv_pad = _pad(kv, ((0, 0), (0, S), (0, 0)))
        kf_pad = _pad(ssm_k, ((0, 0), (0, S)))
        kv_f = (lambda x, axes=(-2,-1): torch.fft.rfft2(x.float(), dim=axes))(kv_pad[:, None, :, :], axes=(1, 2))
        kf_f = (lambda x, axes=(-2,-1): torch.fft.rfft2(x.float(), dim=axes))(kf_pad[:, None, :], axes=(1, 2))
        kf_f = (lambda x, *d: x.permute(*d))(kf_f, 1, 2, 0)
        y_f = kv_f * kf_f[None, :, :, :]
        y = (lambda x, s, axes=(-2,-1): torch.fft.irfft2(x, s=s, dim=axes).to(torch.bfloat16))(y_f, (1, L), axes=(1, 2))[:, 0, :S, :]
        y = y + kv * ssm_D[None, None, :]
        return q * y

    x = A_u
    g = _sigmoid((x * gate_w).sum(-1, keepdims=True))
    a = _A_ref(x, A_log_A_real, A_A_imag, A_C_real, A_C_imag, A_log_dt, A_D_skip)
    b = _B_ref(x, B_k, B_v, B_shift_k, B_ssm_k, B_ssm_D)
    return g * a + (1 - g) * b
```

```cpp
#include <hip/hip_runtime.h>
#include <stdint.h>

typedef unsigned short u16;
typedef unsigned int u32;
typedef unsigned long long u64;
typedef float f32x4 __attribute__((ext_vector_type(4)));
typedef float f32x2 __attribute__((ext_vector_type(2)));
typedef u32 u32x4 __attribute__((ext_vector_type(4)));
typedef u32 u32x2 __attribute__((ext_vector_type(2)));
typedef __bf16 bf16x8 __attribute__((ext_vector_type(8)));
typedef __bf16 bf16x2 __attribute__((ext_vector_type(2)));
#define GAS __attribute__((address_space(1)))
#define LAS __attribute__((address_space(3)))

constexpr int SEQ = 2048, DCH = 1024;
constexpr int US = 4624;
constexpr int U_OFF = 0;
constexpr int XY_OFF = 8 * US;
constexpr int XROW = 544;
constexpr int XCH = 53 * XROW;
constexpr int P_OFF = XY_OFF + 4 * XCH;
constexpr int PS = 644;
constexpr int R_OFF = P_OFF + 4 * PS * 4;
constexpr int LDS_BYTES = R_OFF + 128;
constexpr int KO = 4160;
constexpr int KV = 8320;
constexpr int TS = 136;
constexpr size_t WS_FLAG = 4096, WS_G = 8192, WS_PAY = 65536;

struct Args {
    const u16 *u, *lar, *aim, *cr, *ci, *ldt, *dsk, *k, *v, *shk, *ssk, *ssd, *gw;
    u16* out;
    char* ws;
};

__device__ __forceinline__ float bf2f(u32 h) { return __uint_as_float(h << 16); }
__device__ __forceinline__ float lo2f(u32 d) { return __uint_as_float(d << 16); }
__device__ __forceinline__ float hi2f(u32 d) { return __uint_as_float(d & 0xffff0000u); }
__device__ __forceinline__ u32 pk2(float a, float b) {
    bf16x2 r = {(__bf16)a, (__bf16)b};
    return __builtin_bit_cast(u32, r);
}
__device__ __forceinline__ float rbf(float a) { return lo2f(pk2(a, 0.f)); }
template <int C, int RM>
__device__ __forceinline__ float dpp_add(float v) {
    return v + __int_as_float(__builtin_amdgcn_update_dpp(0, __float_as_int(v), C, RM, 0xf, true));
}
__device__ __forceinline__ float wave_sum63(float v) {
    v = dpp_add<0x111, 0xf>(v); v = dpp_add<0x112, 0xf>(v); v = dpp_add<0x114, 0xf>(v); v = dpp_add<0x118, 0xf>(v);
    v = dpp_add<0x142, 0xa>(v); v = dpp_add<0x143, 0xc>(v);
    return v;
}
__device__ __forceinline__ f32x2 cmul(f32x2 a, f32x2 b) {
    f32x2 r; r.x = a.x * b.x - a.y * b.y; r.y = a.x * b.y + a.y * b.x; return r;
}
__device__ __forceinline__ f32x2 cexp(float ar, float ai, float p) {
    float m = __expf(ar * p);
    float rev = (ai * p) * 0.15915494309189535f;
    rev = __builtin_amdgcn_fractf(rev);
    f32x2 r; r.x = m * __builtin_amdgcn_cosf(rev); r.y = m * __builtin_amdgcn_sinf(rev); return r;
}
#define RLX __ATOMIC_RELAXED, __HIP_MEMORY_SCOPE_AGENT

template <int MODE>
__global__ void __launch_bounds__(512, 2) mix_kernel(Args p) {
    extern __shared__ __attribute__((aligned(16))) char lds[];
    const int tid = threadIdx.x, lane = tid & 63, wave = __builtin_amdgcn_readfirstlane(tid >> 6);
    const int blk = blockIdx.x;
    const int cg = (blk & 7) * 32 + (blk >> 3);
    const __amdgpu_buffer_rsrc_t wsr = __builtin_amdgcn_make_buffer_rsrc((void*)p.ws, 0, 0x7fffffff, 0x00020000);
    u64 tag = 0;
    if (MODE == 0) tag = __hip_atomic_load((GAS u64*)p.ws, RLX) + 1;

    const int gch = tid >> 7, gn = tid & 127;
    float ar, ai; f32x2 X0;
    if (MODE != 1) {
        const int o = (4 * cg + gch) * 128 + gn;
        const float lar = bf2f(p.lar[o]), aim = bf2f(p.aim[o]), cr = bf2f(p.cr[o]), ci = bf2f(p.ci[o]);
        const float dt = rbf(expf(bf2f(p.ldt[4 * cg + gch])));
        ar = -rbf(expf(lar)) * dt; ai = aim * dt;
        X0.x = 2.f * (cr * dt); X0.y = 2.f * (ci * dt);
    }
    const int ph = tid >> 8, pc = tid & 255;
    u32x2 pu[4], pv[4], pk[7], pg; u32x4 pw0, pw1;
    if (MODE != 2) {
        const int t0 = 8 * blk + 4 * ph;
        const GAS u32x2* U = (const GAS u32x2*)p.u; const GAS u32x2* V = (const GAS u32x2*)p.v; const GAS u32x2* K = (const GAS u32x2*)p.k;
#pragma unroll
        for (int i = 0; i < 7; ++i) {
            const int t = t0 - 3 + i;
            u32x2 z = {0u, 0u};
            pk[i] = t >= 0 ? K[t * 256 + pc] : z;
        }
#pragma unroll
        for (int i = 0; i < 4; ++i) { pu[i] = U[(t0 + i) * 256 + pc]; pv[i] = V[(t0 + i) * 256 + pc]; }
        pw0 = ((const GAS u32x4*)p.shk)[2 * pc]; pw1 = ((const GAS u32x4*)p.shk)[2 * pc + 1];
        pg = ((const GAS u32x2*)p.gw)[pc];
    }
    if (MODE != 1) {
        LAS u32* xb = (LAS u32*)(lds + XY_OFF + gch * XCH) + gn;
        constexpr int RW = XROW / 4;
        const f32x2 z = cexp(ar, ai, 1.f), w = cexp(ar, ai, 8.f), w32 = cexp(ar, ai, 256.f), e1024 = cexp(ar, ai, 1024.f);
        f32x2 X[8]; X[0] = X0;
#pragma unroll
        for (int r = 1; r < 8; ++r) X[r] = cmul(X[r - 1], z);
#pragma unroll
        for (int r = 0; r < 8; ++r) xb[r * RW] = pk2(X[r].x, X[r].y);
#pragma unroll
        for (int r = 0; r < 4; ++r) { f32x2 t = cmul(X[2 * r], w32); xb[(8 + r) * RW] = pk2(t.x, t.y); }
        const f32x2 e512 = cmul(w32, w32), e768 = cmul(e512, w32);
        { f32x2 t = cmul(X[0], e512); xb[12 * RW] = pk2(t.x, t.y); t = cmul(X[4], e512); xb[13 * RW] = pk2(t.x, t.y);
          t = cmul(X[0], e768); xb[14 * RW] = pk2(t.x, t.y); t = cmul(X[4], e768); xb[15 * RW] = pk2(t.x, t.y); }
        { const f32x2 x16 = cmul(X[0], e1024); xb[16 * RW] = pk2(x16.x, x16.y);
          f32x2 t = cmul(x16, w32); xb[17 * RW] = pk2(t.x, t.y);
          t = cmul(x16, e512); xb[18 * RW] = pk2(t.x, t.y);
          t = cmul(x16, e768); xb[19 * RW] = pk2(t.x, t.y);
          t = cmul(x16, e1024); xb[20 * RW] = pk2(t.x, t.y); }
        const f32x2 w2 = cmul(w, w);
        f32x2 y0 = {1.f, 0.f}, y1 = w;
#pragma unroll
        for (int j = 0; j < 32; j += 2) {
            xb[(21 + j) * RW] = pk2(y0.x, -y0.y); xb[(22 + j) * RW] = pk2(y1.x, -y1.y);
            y0 = cmul(y0, w2); y1 = cmul(y1, w2);
        }
    }
    if (MODE != 2) {
        float w[4][4];
        w[0][0] = lo2f(pw0.x); w[0][1] = hi2f(pw0.x); w[0][2] = lo2f(pw0.y); w[0][3] = hi2f(pw0.y);
        w[1][0] = lo2f(pw0.z); w[1][1] = hi2f(pw0.z); w[1][2] = lo2f(pw0.w); w[1][3] = hi2f(pw0.w);
        w[2][0] = lo2f(pw1.x); w[2][1] = hi2f(pw1.x); w[2][2] = lo2f(pw1.y); w[2][3] = hi2f(pw1.y);
        w[3][0] = lo2f(pw1.z); w[3][1] = hi2f(pw1.z); w[3][2] = lo2f(pw1.w); w[3][3] = hi2f(pw1.w);
        float kf[7][4], kv[4][4], gs[4];
#pragma unroll
        for (int i = 0; i < 7; ++i) { kf[i][0] = lo2f(pk[i].x); kf[i][1] = hi2f(pk[i].x); kf[i][2] = lo2f(pk[i].y); kf[i][3] = hi2f(pk[i].y); }
        const float g0 = lo2f(pg.x), g1 = hi2f(pg.x), g2 = lo2f(pg.y), g3 = hi2f(pg.y);
#pragma unroll
        for (int i = 0; i < 4; ++i) {
            const float vf[4] = {lo2f(pv[i].x), hi2f(pv[i].x), lo2f(pv[i].y), hi2f(pv[i].y)};
#pragma unroll
            for (int c = 0; c < 4; ++c)
                kv[i][c] = (kf[i][c] * w[c][3] + kf[i + 1][c] * w[c][2] + kf[i + 2][c] * w[c][1] + kf[i + 3][c] * w[c][0]) * vf[c];
            const u32 m01 = pk2(lo2f(pu[i].x) * g0, hi2f(pu[i].x) * g1), m23 = pk2(lo2f(pu[i].y) * g2, hi2f(pu[i].y) * g3);
            gs[i] = wave_sum63((lo2f(m01) + hi2f(m01)) + (lo2f(m23) + hi2f(m23)));
        }
        LAS char* tb = (LAS char*)(lds + U_OFF) + pc * TS + (1 - ph) * 8;
#pragma unroll
        for (int c = 0; c < 4; ++c) {
            u32x2 a, b;
            if (c == 0) { a.x = (pu[3].x & 0xffffu) | (pu[2].x << 16); a.y = (pu[1].x & 0xffffu) | (pu[0].x << 16); }
            if (c == 1) { a.x = (pu[3].x >> 16) | (pu[2].x & 0xffff0000u); a.y = (pu[1].x >> 16) | (pu[0].x & 0xffff0000u); }
            if (c == 2) { a.x = (pu[3].y & 0xffffu) | (pu[2].y << 16); a.y = (pu[1].y & 0xffffu) | (pu[0].y << 16); }
            if (c == 3) { a.x = (pu[3].y >> 16) | (pu[2].y & 0xffff0000u); a.y = (pu[1].y >> 16) | (pu[0].y & 0xffff0000u); }
            b.x = pk2(kv[3][c], kv[2][c]); b.y = pk2(kv[1][c], kv[0][c]);
            *(LAS u32x2*)(tb + c * 16) = a;
            *(LAS u32x2*)(tb + 64 + c * 16) = b;
        }
        if (lane == 63) { f32x4 s = {gs[0], gs[1], gs[2], gs[3]}; *(LAS f32x4*)(lds + R_OFF + wave * 16) = s; }
        __syncthreads();
#pragma unroll
        for (int i = 0; i < 4; ++i) {
            const int P = i * 512 + tid, c = P >> 3, q = P & 7;
            const LAS u32x2* s = (const LAS u32x2*)(lds + U_OFF + c * TS + q * 16);
            const u32x2 a = s[0], b = s[1];
            u32x4 d = {a.x, a.y, b.x, b.y};
            __builtin_amdgcn_raw_buffer_store_b128(d, wsr, (int)WS_PAY + c * 32768 + blk * 128 + q * 16, 0, 16);
        }
        if (wave == 0) {
            const int r = lane & 7, hh = r >> 2, ii = r & 3;
            const LAS float* s = (const LAS float*)(lds + R_OFF) + hh * 16 + ii;
            const float sum = rbf((s[0] + s[4]) + (s[8] + s[12]));
            const float g = 1.f / (1.f + __expf(-sum));
            const u32 gb = pk2(g, 0.f) & 0xffffu;
            u32x4 d;
            d.x = __builtin_amdgcn_readlane(gb, 0) | (__builtin_amdgcn_readlane(gb, 1) << 16);
            d.y = __builtin_amdgcn_readlane(gb, 2) | (__builtin_amdgcn_readlane(gb, 3) << 16);
            d.z = __builtin_amdgcn_readlane(gb, 4) | (__builtin_amdgcn_readlane(gb, 5) << 16);
            d.w = __builtin_amdgcn_readlane(gb, 6) | (__builtin_amdgcn_readlane(gb, 7) << 16);
            if (lane == 0) __builtin_amdgcn_raw_buffer_store_b128(d, wsr, (int)WS_G + blk * 16, 0, 16);
        }
    }
    if (MODE == 1) return;
    u32x4 kb[2]; u32 kbn[2];
#pragma unroll
    for (int i = 0; i < 2; ++i) {
        const int it = i * 512 + tid, ch = it >> 8, q = it & 255;
        const GAS u32* s = (const GAS u32*)(p.ssk + (4 * cg + ch) * SEQ) + 4 * q;
        kb[i] = *(const GAS u32x4*)s;
        kbn[i] = q < 255 ? s[4] : 0u;
    }
    if (MODE == 2) __syncthreads();
    {
        const int ch = wave >> 1, ct = wave & 1;
        const LAS char* xb = (const LAS char*)(lds + XY_OFF + ch * XCH);
        const LAS char* a0 = xb + (lane & 15) * XROW + (lane >> 4) * 16;
        const LAS char* b0 = xb + (21 + ct * 16 + (lane & 15)) * XROW + (lane >> 4) * 16;
        f32x4 d0 = {0.f, 0.f, 0.f, 0.f}, d1 = d0;
#pragma unroll
        for (int ks = 0; ks < 8; ++ks) {
            const bf16x8 fa0 = *(const LAS bf16x8*)(a0 + ks * 64), fa1 = *(const LAS bf16x8*)(a0 + 16 * XROW + ks * 64);
            const bf16x8 fb = *(const LAS bf16x8*)(b0 + ks * 64);
            d0 = __builtin_amdgcn_mfma_f32_16x16x32_bf16(fa0, fb, d0, 0, 0, 0);
            d1 = __builtin_amdgcn_mfma_f32_16x16x32_bf16(fa1, fb, d1, 0, 0, 0);
        }
        LAS float* P = (LAS float*)(lds + P_OFF) + ch * PS;
        const int j = ct * 16 + (lane & 15), rg = lane >> 4;
        if (rg < 2) {
#pragma unroll
            for (int i = 0; i < 4; ++i) P[4 * rg + i + 8 * j] = d0[i];
        } else if (rg == 2) {
#pragma unroll
            for (int i = 0; i < 4; ++i) P[256 + i + 4 * j] = d0[i];
        } else {
            P[384 + 2 * j] = d0[0]; P[385 + 2 * j] = d0[1]; P[448 + 2 * j] = d0[2]; P[449 + 2 * j] = d0[3];
        }
        if (rg == 0) {
#pragma unroll
            for (int i = 0; i < 4; ++i) P[512 + 32 * i + j] = d1[i];
        } else if (rg == 1 && j == 0) P[640] = d1[0];
    }
    if (MODE == 0) {
        asm volatile("s_waitcnt vmcnt(0)" ::: "memory");
    }
    __syncthreads();
    if (MODE == 0 && tid == 0) __hip_atomic_store((GAS u64*)(p.ws + WS_FLAG) + blk, tag, RLX);
#pragma unroll
    for (int i = 0; i < 2; ++i) {
        const int it = i * 512 + tid, ch = it >> 8, q = it & 255;
        const LAS float* P = (const LAS float*)(lds + P_OFF) + ch * PS;
        float k[9];
        if (q < 32) {
#pragma unroll
            for (int e = 0; e < 9; ++e) k[e] = P[8 * q + e];
        } else if (q < 64) {
            const LAS float* s = P + 256 + 4 * (q - 32);
            const float v0 = s[0], v1 = s[1], v2 = s[2], v3 = s[3], v4 = s[4];
            k[0] = v0; k[1] = v0; k[2] = v1; k[3] = v2; k[4] = v2; k[5] = v2; k[6] = v3; k[7] = v4; k[8] = v4;
        } else if (q < 128) {
            const LAS float* s = P + 384 + 2 * (q - 64);
            const float v0 = s[0], v1 = s[1], v2 = s[2];
            k[0] = v0; k[1] = v0; k[2] = v0; k[3] = v1; k[4] = v1; k[5] = v1; k[6] = v2; k[7] = v2; k[8] = v2;
        } else {
            const LAS float* s = P + 512 + (q - 128);
            const float v0 = s[0], v1 = s[1];
            k[0] = v0; k[1] = v0; k[2] = v0; k[3] = v0; k[4] = (q & 1) ? v1 : v0; k[5] = v1; k[6] = v1; k[7] = v1; k[8] = v1;
        }
        LAS char* e = (LAS char*)(lds + XY_OFF + ch * KV) + 32 + q * 16;
        u32x4 de = {pk2(k[0], k[1]), pk2(k[2], k[3]), pk2(k[4], k[5]), pk2(k[6], k[7])};
        u32x4 dz = {pk2(k[1], k[2]), pk2(k[3], k[4]), pk2(k[5], k[6]), pk2(k[7], k[8])};
        *(LAS u32x4*)e = de; *(LAS u32x4*)(e + KO) = dz;
        LAS char* f = e + 4 * KV;
        u32x4 dy = {__builtin_amdgcn_alignbit(kb[i].y, kb[i].x, 16), __builtin_amdgcn_alignbit(kb[i].z, kb[i].y, 16),
                    __builtin_amdgcn_alignbit(kb[i].w, kb[i].z, 16), __builtin_amdgcn_alignbit(kbn[i], kb[i].w, 16)};
        *(LAS u32x4*)f = kb[i]; *(LAS u32x4*)(f + KO) = dy;
        if (q == 0) {
            const u32x4 z = {0u, 0u, 0u, 0u};
            u32x4 ha = {0u, 0u, 0u, pk2(0.f, k[0])}, hb = {0u, 0u, 0u, kb[i].x << 16};
            *(LAS u32x4*)(e - 32) = z; *(LAS u32x4*)(e - 16) = z; *(LAS u32x4*)(e + KO - 32) = z; *(LAS u32x4*)(e + KO - 16) = ha;
            *(LAS u32x4*)(f - 32) = z; *(LAS u32x4*)(f - 16) = z; *(LAS u32x4*)(f + KO - 32) = z; *(LAS u32x4*)(f + KO - 16) = hb;
        }
    }
    if (tid < 240) { const u32x4 z = {0u, 0u, 0u, 0u}; *(LAS u32x4*)(lds + U_OFF + (tid / 30) * US + 4096 + (tid % 30) * 16) = z; }
    if (MODE == 0) {
        if (wave == 0) {
            const GAS u64* fl = (const GAS u64*)(p.ws + WS_FLAG);
            for (u32 spins = 0; spins < (1u << 20); ++spins) {
                const u64 f0 = __hip_atomic_load(fl + lane, RLX), f1 = __hip_atomic_load(fl + 64 + lane, RLX);
                const u64 f2 = __hip_atomic_load(fl + 128 + lane, RLX), f3 = __hip_atomic_load(fl + 192 + lane, RLX);
                if (__all(f0 == tag && f1 == tag && f2 == tag && f3 == tag)) break;
                __builtin_amdgcn_s_sleep(2);
            }
            if (blk == 0 && lane == 0) __hip_atomic_store((GAS u64*)p.ws, tag, RLX);
        }
        __syncthreads();
    }
    u32x2 gq;
    {
        u32x4 d[4];
#pragma unroll
        for (int i = 0; i < 4; ++i) d[i] = __builtin_amdgcn_raw_buffer_load_b128(wsr, (int)WS_PAY + cg * 32768 + (i * 512 + tid) * 16, 0, 16);
        gq = __builtin_amdgcn_raw_buffer_load_b64(wsr, (int)WS_G + tid * 8, 0, 16);
#pragma unroll
        for (int i = 0; i < 4; ++i) {
            const int P = i * 512 + tid, b = P >> 3, q = P & 7;
            *(LAS u32x4*)(lds + U_OFF + q * US + 4080 - 16 * b) = d[i];
        }
    }
    const float sk = bf2f(wave < 4 ? p.dsk[4 * cg + wave] : p.ssd[4 * cg + wave - 4]);
    __syncthreads();
    f32x4 acc[8];
#pragma unroll
    for (int c = 0; c < 8; ++c) acc[c] = f32x4{0.f, 0.f, 0.f, 0.f};
    {
        const int j = lane & 15, kg = lane >> 4;
        const LAS char* ab = (const LAS char*)(lds + XY_OFF + wave * KV) + ((j & 1) ? 2 * (1 + j + 8 * kg) : KO + 2 * (j + 8 * kg));
        const LAS char* bb = (const LAS char*)(lds + U_OFF + wave * US) + 2 * (2032 - 16 * j + 8 * kg);
#pragma unroll
        for (int r = 0; r < 8; ++r) {
            bf16x8 A[8], B[8];
#pragma unroll
            for (int a = 0; a < 8; ++a) {
                const volatile LAS u32* s = (const volatile LAS u32*)(ab + 512 * a + 64 * r);
                u32x4 t = {s[0], s[1], s[2], s[3]};
                A[a] = __builtin_bit_cast(bf16x8, t);
                B[a] = *(const LAS bf16x8*)(bb - 512 * a + 64 * r);
            }
#pragma unroll
            for (int b = 0; b < 8; ++b)
#pragma unroll
                for (int a = 0; a + b < 8; ++a) acc[a + b] = __builtin_amdgcn_mfma_f32_16x16x32_bf16(A[a], B[b], acc[a + b], 0, 0, 0);
        }
    }
    {
        const int n = lane & 15, kg = lane >> 4;
        const int sg = 4 * n + kg, ps = sg ^ ((sg >> 3) & 7);
        const int cu = wave & 3;
        LAS char* rb = (LAS char*)(lds + XY_OFF + wave * KV) + ps * 16;
#pragma unroll
        for (int c = 0; c < 8; ++c) {
            const int t0 = 256 * c + 16 * n + 4 * kg;
            const u32x2 uu = *(const LAS u32x2*)(lds + U_OFF + cu * US + 2 * (2044 - t0));
            const float u0 = hi2f(uu.y), u1 = lo2f(uu.y), u2 = hi2f(uu.x), u3 = lo2f(uu.x);
            f32x4 r;
            if (wave < 4) {
                r.x = acc[c].x + u0 * sk; r.y = acc[c].y + u1 * sk; r.z = acc[c].z + u2 * sk; r.w = acc[c].w + u3 * sk;
            } else {
                const u32x2 kk = *(const LAS u32x2*)(lds + U_OFF + wave * US + 2 * (2044 - t0));
                r.x = u0 * (acc[c].x + hi2f(kk.y) * sk); r.y = u1 * (acc[c].y + lo2f(kk.y) * sk);
                r.z = u2 * (acc[c].z + hi2f(kk.x) * sk); r.w = u3 * (acc[c].w + lo2f(kk.x) * sk);
            }
            *(LAS f32x4*)(rb + c * 1024) = r;
        }
    }
    __syncthreads();
    {
        const int ps = lane ^ ((lane >> 3) & 7);
        const LAS char* rb = (const LAS char*)(lds + XY_OFF) + wave * 1024 + ps * 16;
        const float g[4] = {lo2f(gq.x), hi2f(gq.x), lo2f(gq.y), hi2f(gq.y)};
        float o[4][4];
#pragma unroll
        for (int c = 0; c < 4; ++c) {
            const f32x4 a = *(const LAS f32x4*)(rb + c * KV), b = *(const LAS f32x4*)(rb + (4 + c) * KV);
#pragma unroll
            for (int i = 0; i < 4; ++i) o[i][c] = g[i] * a[i] + (1.f - g[i]) * b[i];
        }
#pragma unroll
        for (int i = 0; i < 4; ++i) {
            u32x2 d = {pk2(o[i][0], o[i][1]), pk2(o[i][2], o[i][3])};
            *(GAS u32x2*)(p.out + (size_t)(4 * tid + i) * DCH + 4 * cg) = d;
        }
    }
}

extern "C" void kernel_launch(void* const* d_in, const int* in_sizes, int n_in, void* d_out, int out_size, void* d_ws,
                              size_t ws_size, hipStream_t stream) {
    Args a;
    a.u = (const u16*)d_in[0]; a.lar = (const u16*)d_in[1]; a.aim = (const u16*)d_in[2]; a.cr = (const u16*)d_in[3];
    a.ci = (const u16*)d_in[4]; a.ldt = (const u16*)d_in[5]; a.dsk = (const u16*)d_in[6]; a.k = (const u16*)d_in[8];
    a.v = (const u16*)d_in[9]; a.shk = (const u16*)d_in[10]; a.ssk = (const u16*)d_in[11]; a.ssd = (const u16*)d_in[12];
    a.gw = (const u16*)d_in[13]; a.out = (u16*)d_out; a.ws = (char*)d_ws;
    (void)hipFuncSetAttribute((const void*)mix_kernel<0>, hipFuncAttributeMaxDynamicSharedMemorySize, LDS_BYTES);
    mix_kernel<0><<<256, 512, LDS_BYTES, stream>>>(a);
}
```

```cpp
#include <hip/hip_runtime.h>
#include <stdint.h>

typedef unsigned short u16;
typedef unsigned int u32;
typedef unsigned long long u64;
typedef float f32x4 __attribute__((ext_vector_type(4)));
typedef float f32x2 __attribute__((ext_vector_type(2)));
typedef u32 u32x4 __attribute__((ext_vector_type(4)));
typedef u32 u32x2 __attribute__((ext_vector_type(2)));
typedef __bf16 bf16x8 __attribute__((ext_vector_type(8)));
typedef __bf16 bf16x2 __attribute__((ext_vector_type(2)));
#define GAS __attribute__((address_space(1)))
#define LAS __attribute__((address_space(3)))

constexpr int SEQ = 2048, DCH = 1024;
constexpr int US = 4624;
constexpr int U_OFF = 0;
constexpr int XY_OFF = 8 * US;
constexpr int XROW = 544;
constexpr int XCH = 53 * XROW;
constexpr int P_OFF = XY_OFF + 4 * XCH;
constexpr int PS = 644;
constexpr int R_OFF = P_OFF + 4 * PS * 4;
constexpr int LDS_BYTES = R_OFF + 128;
constexpr int KO = 4160;
constexpr int KV = 8320;
constexpr int TS = 136;
constexpr size_t WS_FLAG = 4096, WS_G = 8192, WS_PAY = 65536;

struct Args {
    const u16 *u, *lar, *aim, *cr, *ci, *ldt, *dsk, *k, *v, *shk, *ssk, *ssd, *gw;
    u16* out;
    char* ws;
};

__device__ __forceinline__ float bf2f(u32 h) { return __uint_as_float(h << 16); }
__device__ __forceinline__ float lo2f(u32 d) { return __uint_as_float(d << 16); }
__device__ __forceinline__ float hi2f(u32 d) { return __uint_as_float(d & 0xffff0000u); }
__device__ __forceinline__ u32 pk2(float a, float b) {
    bf16x2 r = {(__bf16)a, (__bf16)b};
    return __builtin_bit_cast(u32, r);
}
__device__ __forceinline__ float rbf(float a) { return lo2f(pk2(a, 0.f)); }
template <int C, int RM>
__device__ __forceinline__ float dpp_add(float v) {
    return v + __int_as_float(__builtin_amdgcn_update_dpp(0, __float_as_int(v), C, RM, 0xf, true));
}
__device__ __forceinline__ float wave_sum63(float v) {
    v = dpp_add<0x111, 0xf>(v); v = dpp_add<0x112, 0xf>(v); v = dpp_add<0x114, 0xf>(v); v = dpp_add<0x118, 0xf>(v);
    v = dpp_add<0x142, 0xa>(v); v = dpp_add<0x143, 0xc>(v);
    return v;
}
__device__ __forceinline__ f32x2 cmul(f32x2 a, f32x2 b) {
    f32x2 r;
    asm("v_pk_mul_f32 %0, %1, %2 op_sel:[0,0] op_sel_hi:[0,1]" : "=v"(r) : "v"(a), "v"(b));
    asm("v_pk_fma_f32 %0, %1, %2, %0 op_sel:[1,1,0] op_sel_hi:[1,0,1] neg_lo:[0,1,0]" : "+v"(r) : "v"(a), "v"(b));
    return r;
}
__device__ __forceinline__ f32x2 cexp(float ar, float ai, float p) {
    float m = __expf(ar * p);
    float rev = (ai * p) * 0.15915494309189535f;
    rev = __builtin_amdgcn_fractf(rev);
    f32x2 r; r.x = m * __builtin_amdgcn_cosf(rev); r.y = m * __builtin_amdgcn_sinf(rev); return r;
}
#define RLX __ATOMIC_RELAXED, __HIP_MEMORY_SCOPE_AGENT
constexpr size_t WS_PROF = 32u << 20;
#define STAMP(i)

#define AQ
#define NDS 3
template <int MODE>
__global__ void __launch_bounds__(512, 2) mix_kernel(Args p) {
    extern __shared__ __attribute__((aligned(16))) char lds[];
    const int tid = threadIdx.x, lane = tid & 63, wave = __builtin_amdgcn_readfirstlane(tid >> 6);
    const int blk = blockIdx.x;
    const int cg = (blk & 7) * 32 + (blk >> 3);
    const __amdgpu_buffer_rsrc_t wsr = __builtin_amdgcn_make_buffer_rsrc((void*)p.ws, 0, 0x7fffffff, 0x00020000);
    STAMP(0);
    u64 tag = 0;
    if (MODE == 0) tag = __hip_atomic_load((GAS u64*)p.ws, RLX) + 1;

    const int gch = tid >> 7, gn = tid & 127;
    const int ph = tid >> 8, pc = tid & 255;
    u32x2 pu[4], pv[4], pk[7], pg; u32x4 pw0, pw1;
    u32 q_lar, q_aim, q_cr, q_ci, q_ldt;
    if (MODE != 2) {
        const int t0 = 8 * blk + 4 * ph;
        const GAS u32x2* U = (const GAS u32x2*)p.u; const GAS u32x2* V = (const GAS u32x2*)p.v; const GAS u32x2* K = (const GAS u32x2*)p.k;
#pragma unroll
        for (int i = 0; i < 7; ++i) {
            const int t = t0 - 3 + i;
            pk[i] = K[(t < 0 ? 0 : t) * 256 + pc];
        }
#pragma unroll
        for (int i = 0; i < 4; ++i) { pv[i] = V[(t0 + i) * 256 + pc]; pu[i] = U[(t0 + i) * 256 + pc]; }
        pw0 = ((const GAS u32x4*)p.shk)[2 * pc]; pw1 = ((const GAS u32x4*)p.shk)[2 * pc + 1];
        pg = ((const GAS u32x2*)p.gw)[pc];
    }
    u32x4 kb[2]; u32 kbn[2]; u32 q_sk[2];
    if (MODE != 1) {
        const int o = (4 * cg + gch) * 128 + gn;
        q_lar = p.lar[o]; q_aim = p.aim[o]; q_cr = p.cr[o]; q_ci = p.ci[o]; q_ldt = p.ldt[4 * cg + gch];
#pragma unroll
        for (int i = 0; i < 2; ++i) {
            const int it = i * 512 + tid, ch = it >> 8, q = it & 255;
            const GAS u32* s = (const GAS u32*)(p.ssk + (4 * cg + ch) * SEQ) + 4 * q;
            kb[i] = *(const GAS u32x4*)s;
            kbn[i] = s[q < 255 ? 4 : 0];
            q_sk[i] = p.ssd[4 * cg + ch];
        }
    }
    const u32 q_dsk0 = p.dsk[4 * cg + (wave >> 1)];
    __builtin_amdgcn_sched_barrier(0);
    STAMP(1);
    if (MODE != 2) {
        if (blk == 0 && ph == 0) { pk[0] = u32x2{0u, 0u}; pk[1] = pk[0]; pk[2] = pk[0]; }
        f32x2 wa[4], wb[4];
        wa[0] = f32x2{lo2f(pw0.x), lo2f(pw0.z)}; wa[1] = f32x2{hi2f(pw0.x), hi2f(pw0.z)};
        wa[2] = f32x2{lo2f(pw0.y), lo2f(pw0.w)}; wa[3] = f32x2{hi2f(pw0.y), hi2f(pw0.w)};
        wb[0] = f32x2{lo2f(pw1.x), lo2f(pw1.z)}; wb[1] = f32x2{hi2f(pw1.x), hi2f(pw1.z)};
        wb[2] = f32x2{lo2f(pw1.y), lo2f(pw1.w)}; wb[3] = f32x2{hi2f(pw1.y), hi2f(pw1.w)};
        f32x2 ka[7], kc[7], kva[4], kvc[4]; float gs[4];
#pragma unroll
        for (int i = 0; i < 7; ++i) { ka[i] = f32x2{lo2f(pk[i].x), hi2f(pk[i].x)}; kc[i] = f32x2{lo2f(pk[i].y), hi2f(pk[i].y)}; }
        const f32x2 ga = {lo2f(pg.x), hi2f(pg.x)}, gc = {lo2f(pg.y), hi2f(pg.y)};
#pragma unroll
        for (int i = 0; i < 4; ++i) {
            const f32x2 va = {lo2f(pv[i].x), hi2f(pv[i].x)}, vc = {lo2f(pv[i].y), hi2f(pv[i].y)};
            kva[i] = (ka[i] * wa[3] + ka[i + 1] * wa[2] + ka[i + 2] * wa[1] + ka[i + 3] * wa[0]) * va;
            kvc[i] = (kc[i] * wb[3] + kc[i + 1] * wb[2] + kc[i + 2] * wb[1] + kc[i + 3] * wb[0]) * vc;
            const f32x2 ua = {lo2f(pu[i].x), hi2f(pu[i].x)}, uc = {lo2f(pu[i].y), hi2f(pu[i].y)};
            const f32x2 m = ua * ga + uc * gc;
            gs[i] = m.x + m.y;
        }
        LAS char* tb = (LAS char*)(lds + U_OFF) + pc * TS + (1 - ph) * 8;
        constexpr u32 LO = 0x05040100u, HI = 0x07060302u;
        *(LAS u32x2*)(tb + 0) = u32x2{__builtin_amdgcn_perm(pu[2].x, pu[3].x, LO), __builtin_amdgcn_perm(pu[0].x, pu[1].x, LO)};
        *(LAS u32x2*)(tb + 16) = u32x2{__builtin_amdgcn_perm(pu[2].x, pu[3].x, HI), __builtin_amdgcn_perm(pu[0].x, pu[1].x, HI)};
        *(LAS u32x2*)(tb + 32) = u32x2{__builtin_amdgcn_perm(pu[2].y, pu[3].y, LO), __builtin_amdgcn_perm(pu[0].y, pu[1].y, LO)};
        *(LAS u32x2*)(tb + 48) = u32x2{__builtin_amdgcn_perm(pu[2].y, pu[3].y, HI), __builtin_amdgcn_perm(pu[0].y, pu[1].y, HI)};
        *(LAS u32x2*)(tb + 64) = u32x2{pk2(kva[3].x, kva[2].x), pk2(kva[1].x, kva[0].x)};
        *(LAS u32x2*)(tb + 80) = u32x2{pk2(kva[3].y, kva[2].y), pk2(kva[1].y, kva[0].y)};
        *(LAS u32x2*)(tb + 96) = u32x2{pk2(kvc[3].x, kvc[2].x), pk2(kvc[1].x, kvc[0].x)};
        *(LAS u32x2*)(tb + 112) = u32x2{pk2(kvc[3].y, kvc[2].y), pk2(kvc[1].y, kvc[0].y)};
#pragma unroll
        for (int i = 0; i < 4; ++i) gs[i] = dpp_add<0x111, 0xf>(gs[i]);
#pragma unroll
        for (int i = 0; i < 4; ++i) gs[i] = dpp_add<0x112, 0xf>(gs[i]);
#pragma unroll
        for (int i = 0; i < 4; ++i) gs[i] = dpp_add<0x114, 0xf>(gs[i]);
#pragma unroll
        for (int i = 0; i < 4; ++i) gs[i] = dpp_add<0x118, 0xf>(gs[i]);
#pragma unroll
        for (int i = 0; i < 4; ++i) gs[i] = dpp_add<0x142, 0xa>(gs[i]);
#pragma unroll
        for (int i = 0; i < 4; ++i) gs[i] = dpp_add<0x143, 0xc>(gs[i]);
        if (lane == 63) { f32x4 s = {gs[0], gs[1], gs[2], gs[3]}; *(LAS f32x4*)(lds + R_OFF + wave * 16) = s; }
        STAMP(2);
        __syncthreads();
        STAMP(3);
#pragma unroll
        for (int i = 0; i < 4; ++i) {
            const int P = i * 512 + tid, c = P >> 3, q = P & 7;
            const LAS u32x2* s = (const LAS u32x2*)(lds + U_OFF + c * TS + q * 16);
            const u32x2 a = s[0], b = s[1];
            u32x4 d = {a.x, a.y, b.x, b.y};
            __builtin_amdgcn_raw_buffer_store_b128(d, wsr, (int)WS_PAY + c * 32768 + blk * 128 + q * 16, 0, 16);
        }
        if (wave == 7) {
            const int r = lane & 7, hh = r >> 2, ii = r & 3;
            const LAS float* s = (const LAS float*)(lds + R_OFF) + hh * 16 + ii;
            const float sum = rbf((s[0] + s[4]) + (s[8] + s[12]));
            const float g = __builtin_amdgcn_rcpf(1.f + __expf(-sum));
            const u32 gb = pk2(g, 0.f) & 0xffffu;
            u32x4 d;
            d.x = __builtin_amdgcn_readlane(gb, 0) | (__builtin_amdgcn_readlane(gb, 1) << 16);
            d.y = __builtin_amdgcn_readlane(gb, 2) | (__builtin_amdgcn_readlane(gb, 3) << 16);
            d.z = __builtin_amdgcn_readlane(gb, 4) | (__builtin_amdgcn_readlane(gb, 5) << 16);
            d.w = __builtin_amdgcn_readlane(gb, 6) | (__builtin_amdgcn_readlane(gb, 7) << 16);
            if (lane == 0) __builtin_amdgcn_raw_buffer_store_b128(d, wsr, (int)WS_G + blk * 16, 0, 16);
        }
    }
    STAMP(4);
    if (MODE == 1) return;
    {
        const float dt = rbf(expf(bf2f(q_ldt)));
        const float ar = -rbf(expf(bf2f(q_lar))) * dt, ai = bf2f(q_aim) * dt;
        const f32x2 X0 = {2.f * (bf2f(q_cr) * dt), 2.f * (bf2f(q_ci) * dt)};
        LAS u32* xb = (LAS u32*)(lds + XY_OFF + gch * XCH) + gn;
        constexpr int RW = XROW / 4;
        const f32x2 z = cexp(ar, ai, 1.f), w = cexp(ar, ai, 8.f), w32 = cexp(ar, ai, 256.f), e1024 = cexp(ar, ai, 1024.f);
        f32x2 X[8]; X[0] = X0;
#pragma unroll
        for (int r = 1; r < 8; ++r) X[r] = cmul(X[r - 1], z);
#pragma unroll
        for (int r = 0; r < 8; ++r) xb[r * RW] = pk2(X[r].x, X[r].y);
#pragma unroll
        for (int r = 0; r < 4; ++r) { f32x2 t = cmul(X[2 * r], w32); xb[(8 + r) * RW] = pk2(t.x, t.y); }
        const f32x2 e512 = cmul(w32, w32), e768 = cmul(e512, w32);
        { f32x2 t = cmul(X[0], e512); xb[12 * RW] = pk2(t.x, t.y); t = cmul(X[4], e512); xb[13 * RW] = pk2(t.x, t.y);
          t = cmul(X[0], e768); xb[14 * RW] = pk2(t.x, t.y); t = cmul(X[4], e768); xb[15 * RW] = pk2(t.x, t.y); }
        { const f32x2 x16 = cmul(X[0], e1024); xb[16 * RW] = pk2(x16.x, x16.y);
          f32x2 t = cmul(x16, w32); xb[17 * RW] = pk2(t.x, t.y);
          t = cmul(x16, e512); xb[18 * RW] = pk2(t.x, t.y);
          t = cmul(x16, e768); xb[19 * RW] = pk2(t.x, t.y);
          t = cmul(x16, e1024); xb[20 * RW] = pk2(t.x, t.y); }
        const f32x2 wc = {w.x, -w.y};
        const f32x2 w2 = cmul(wc, wc);
        f32x2 y0 = {1.f, 0.f}, y1 = wc;
#pragma unroll
        for (int j = 0; j < 32; j += 2) {
            xb[(21 + j) * RW] = pk2(y0.x, y0.y); xb[(22 + j) * RW] = pk2(y1.x, y1.y);
            if (j < 30) { y0 = cmul(y0, w2); y1 = cmul(y1, w2); }
        }
    }
    STAMP(5);
    if (MODE == 0) asm volatile("s_waitcnt vmcnt(0)" ::: "memory");
    __syncthreads();
    if (MODE == 0 && tid == 0) __hip_atomic_store((GAS u64*)(p.ws + WS_FLAG) + blk, tag, RLX);
    STAMP(6);
    {
        const int ch = wave >> 1, ct = wave & 1;
        const LAS char* xb = (const LAS char*)(lds + XY_OFF + ch * XCH);
        const LAS char* a0 = xb + (lane & 15) * XROW + (lane >> 4) * 16;
        const LAS char* b0 = xb + (21 + ct * 16 + (lane & 15)) * XROW + (lane >> 4) * 16;
        f32x4 d0 = {0.f, 0.f, 0.f, 0.f}, d1 = d0;
#pragma unroll
        for (int ks = 0; ks < 8; ++ks) {
            const bf16x8 fa0 = *(const LAS bf16x8*)(a0 + ks * 64), fa1 = *(const LAS bf16x8*)(a0 + 16 * XROW + ks * 64);
            const bf16x8 fb = *(const LAS bf16x8*)(b0 + ks * 64);
            d0 = __builtin_amdgcn_mfma_f32_16x16x32_bf16(fa0, fb, d0, 0, 0, 0);
            d1 = __builtin_amdgcn_mfma_f32_16x16x32_bf16(fa1, fb, d1, 0, 0, 0);
        }
        LAS float* P = (LAS float*)(lds + P_OFF) + ch * PS;
        const int j = ct * 16 + (lane & 15), rg = lane >> 4;
        if (lane == 0 && ct == 0) d0[0] += bf2f(q_dsk0);
        if (rg < 2) {
#pragma unroll
            for (int i = 0; i < 4; ++i) P[4 * rg + i + 8 * j] = d0[i];
        } else if (rg == 2) {
#pragma unroll
            for (int i = 0; i < 4; ++i) P[256 + i + 4 * j] = d0[i];
        } else {
            P[384 + 2 * j] = d0[0]; P[385 + 2 * j] = d0[1]; P[448 + 2 * j] = d0[2]; P[449 + 2 * j] = d0[3];
        }
        if (rg == 0) {
#pragma unroll
            for (int i = 0; i < 4; ++i) P[512 + 32 * i + j] = d1[i];
        } else if (rg == 1 && j == 0) P[640] = d1[0];
    }
    __syncthreads();
    STAMP(7);
#pragma unroll
    for (int i = 0; i < 2; ++i) {
        const int it = i * 512 + tid, ch = it >> 8, q = it & 255;
        const LAS float* P = (const LAS float*)(lds + P_OFF) + ch * PS;
        float k[9];
        if (q < 32) {
#pragma unroll
            for (int e = 0; e < 9; ++e) k[e] = P[8 * q + e];
        } else if (q < 64) {
            const LAS float* s = P + 256 + 4 * (q - 32);
            const float v0 = s[0], v1 = s[1], v2 = s[2], v3 = s[3], v4 = s[4];
            k[0] = v0; k[1] = v0; k[2] = v1; k[3] = v2; k[4] = v2; k[5] = v2; k[6] = v3; k[7] = v4; k[8] = v4;
        } else if (q < 128) {
            const LAS float* s = P + 384 + 2 * (q - 64);
            const float v0 = s[0], v1 = s[1], v2 = s[2];
            k[0] = v0; k[1] = v0; k[2] = v0; k[3] = v1; k[4] = v1; k[5] = v1; k[6] = v2; k[7] = v2; k[8] = v2;
        } else {
            const LAS float* s = P + 512 + (q - 128);
            const float v0 = s[0], v1 = s[1];
            k[0] = v0; k[1] = v0; k[2] = v0; k[3] = v0; k[4] = (q & 1) ? v1 : v0; k[5] = v1; k[6] = v1; k[7] = v1; k[8] = v1;
        }
        LAS char* e = (LAS char*)(lds + XY_OFF + ch * KV) + 32 + q * 16;
        u32x4 de = {pk2(k[0], k[1]), pk2(k[2], k[3]), pk2(k[4], k[5]), pk2(k[6], k[7])};
        u32x4 dz = {pk2(k[1], k[2]), pk2(k[3], k[4]), pk2(k[5], k[6]), pk2(k[7], k[8])};
        *(LAS u32x4*)e = de; *(LAS u32x4*)(e + KO) = dz;
        LAS char* f = e + 4 * KV;
        if (q == 0) kb[i].x = (kb[i].x & 0xffff0000u) | (pk2(lo2f(kb[i].x) + bf2f(q_sk[i]), 0.f) & 0xffffu);
        u32x4 dy = {__builtin_amdgcn_alignbit(kb[i].y, kb[i].x, 16), __builtin_amdgcn_alignbit(kb[i].z, kb[i].y, 16),
                    __builtin_amdgcn_alignbit(kb[i].w, kb[i].z, 16), __builtin_amdgcn_alignbit(kbn[i], kb[i].w, 16)};
        *(LAS u32x4*)f = kb[i]; *(LAS u32x4*)(f + KO) = dy;
        if (q == 0) {
            const u32x4 z = {0u, 0u, 0u, 0u};
            u32x4 ha = {0u, 0u, 0u, pk2(0.f, k[0])}, hb = {0u, 0u, 0u, kb[i].x << 16};
            *(LAS u32x4*)(e - 32) = z; *(LAS u32x4*)(e - 16) = z; *(LAS u32x4*)(e + KO - 32) = z; *(LAS u32x4*)(e + KO - 16) = ha;
            *(LAS u32x4*)(f - 32) = z; *(LAS u32x4*)(f - 16) = z; *(LAS u32x4*)(f + KO - 32) = z; *(LAS u32x4*)(f + KO - 16) = hb;
        }
    }
    if (tid < 240) { const u32x4 z = {0u, 0u, 0u, 0u}; *(LAS u32x4*)(lds + U_OFF + (tid / 30) * US + 4096 + (tid % 30) * 16) = z; }
    STAMP(8);
    if (MODE == 0) {
        if (wave == 0) {
            const GAS u64* fl = (const GAS u64*)(p.ws + WS_FLAG);
            for (u32 spins = 0; spins < (1u << 20); ++spins) {
                const u64 f0 = __hip_atomic_load(fl + lane, RLX), f1 = __hip_atomic_load(fl + 64 + lane, RLX);
                const u64 f2 = __hip_atomic_load(fl + 128 + lane, RLX), f3 = __hip_atomic_load(fl + 192 + lane, RLX);
                if (__all(f0 == tag && f1 == tag && f2 == tag && f3 == tag)) break;
                __builtin_amdgcn_s_sleep(2);
            }
            if (blk == 0 && lane == 0) __hip_atomic_store((GAS u64*)p.ws, tag, RLX);
        }
        __syncthreads();
    }
    STAMP(9);
    u32x2 gq;
    {
        u32x4 d[4];
#pragma unroll
        for (int i = 0; i < 4; ++i) d[i] = __builtin_amdgcn_raw_buffer_load_b128(wsr, (int)WS_PAY + cg * 32768 + (i * 512 + tid) * 16, 0, 16);
        gq = __builtin_amdgcn_raw_buffer_load_b64(wsr, (int)WS_G + tid * 8, 0, 16);
#pragma unroll
        for (int i = 0; i < 4; ++i) {
            const int P = i * 512 + tid, b = P >> 3, q = P & 7;
            *(LAS u32x4*)(lds + U_OFF + q * US + 4080 - 16 * b) = d[i];
        }
    }
    __syncthreads();
    STAMP(10);
    f32x4 acc[8];
#pragma unroll
    for (int c = 0; c < 8; ++c) acc[c] = f32x4{0.f, 0.f, 0.f, 0.f};
    {
        const int j = lane & 15, kg = lane >> 4;
        const LAS char* ab = (const LAS char*)(lds + XY_OFF + wave * KV) + ((j & 1) ? 2 * (1 + j + 8 * kg) : KO + 2 * (j + 8 * kg));
        const LAS char* bb = (const LAS char*)(lds + U_OFF + wave * US) + 2 * (2032 - 16 * j + 8 * kg);
        bf16x8 A[2][8], B[2][8];
        auto ld = [&](int r, bf16x8* a_, bf16x8* b_) {
#pragma unroll
            for (int a = 0; a < 8; ++a) {
                const AQ LAS u32* s = (const AQ LAS u32*)(ab + 512 * a + 64 * r);
                u32x4 t = {s[0], s[1], s[2], s[3]};
                a_[a] = __builtin_bit_cast(bf16x8, t);
                b_[a] = *(const LAS bf16x8*)(bb - 512 * a + 64 * r);
            }
        };
        ld(0, A[0], B[0]);
#pragma unroll
        for (int r = 0; r < 8; ++r) {
            if (r < 7) ld(r + 1, A[(r + 1) & 1], B[(r + 1) & 1]);
#pragma unroll
            for (int b = 0; b < 8; ++b)
#pragma unroll
                for (int a = 0; a + b < 8; ++a)
                    acc[a + b] = __builtin_amdgcn_mfma_f32_16x16x32_bf16(A[r & 1][a], B[r & 1][b], acc[a + b], 0, 0, 0);
            if (r < 7) {
#pragma unroll
                for (int i = 0; i < 8; ++i) {
                    __builtin_amdgcn_sched_group_barrier(0x008, 2, 0);
                    __builtin_amdgcn_sched_group_barrier(0x100, NDS, 0);
                    __builtin_amdgcn_sched_group_barrier(0x008, 2, 0);
                }
                __builtin_amdgcn_sched_group_barrier(0x008, 4, 0);
            }
            __builtin_amdgcn_sched_barrier(0);
        }
    }
    STAMP(11);
    {
        const int n = lane & 15, kg = lane >> 4;
        const int sg = 4 * n + kg, ps = sg ^ ((sg >> 3) & 7);
        LAS char* rb = (LAS char*)(lds + XY_OFF + wave * KV) + ps * 16;
#pragma unroll
        for (int c = 0; c < 8; ++c) *(LAS f32x4*)(rb + c * 1024) = acc[c];
    }
    __syncthreads();
    STAMP(12);
    {
        const int ps = lane ^ ((lane >> 3) & 7);
        const LAS char* rb = (const LAS char*)(lds + XY_OFF) + wave * 1024 + ps * 16;
        const LAS char* ub = (const LAS char*)(lds + U_OFF) + 2 * (2044 - 4 * tid);
        const f32x2 g01 = {lo2f(gq.x), hi2f(gq.x)}, g23 = {lo2f(gq.y), hi2f(gq.y)};
        f32x2 o01[4], o23[4];
#pragma unroll
        for (int c = 0; c < 4; ++c) {
            const f32x4 a = *(const LAS f32x4*)(rb + c * KV), b = *(const LAS f32x4*)(rb + (4 + c) * KV);
            const u32x2 uu = *(const LAS u32x2*)(ub + c * US);
            const f32x2 u01 = {hi2f(uu.y), lo2f(uu.y)}, u23 = {hi2f(uu.x), lo2f(uu.x)};
            const f32x2 b01 = u01 * f32x2{b.x, b.y}, b23 = u23 * f32x2{b.z, b.w};
            o01[c] = g01 * (f32x2{a.x, a.y} - b01) + b01;
            o23[c] = g23 * (f32x2{a.z, a.w} - b23) + b23;
        }
        GAS char* ob = (GAS char*)(p.out + (size_t)(4 * tid) * DCH + 4 * cg);
        *(GAS u32x2*)(ob) = u32x2{pk2(o01[0].x, o01[1].x), pk2(o01[2].x, o01[3].x)};
        *(GAS u32x2*)(ob + 2 * DCH) = u32x2{pk2(o01[0].y, o01[1].y), pk2(o01[2].y, o01[3].y)};
        *(GAS u32x2*)(ob + 4 * DCH) = u32x2{pk2(o23[0].x, o23[1].x), pk2(o23[2].x, o23[3].x)};
        *(GAS u32x2*)(ob + 6 * DCH) = u32x2{pk2(o23[0].y, o23[1].y), pk2(o23[2].y, o23[3].y)};
    }
    STAMP(13);
}

extern "C" void kernel_launch(void* const* d_in, const int* in_sizes, int n_in, void* d_out, int out_size, void* d_ws,
                              size_t ws_size, hipStream_t stream) {
    Args a;
    a.u = (const u16*)d_in[0]; a.lar = (const u16*)d_in[1]; a.aim = (const u16*)d_in[2]; a.cr = (const u16*)d_in[3];
    a.ci = (const u16*)d_in[4]; a.ldt = (const u16*)d_in[5]; a.dsk = (const u16*)d_in[6]; a.k = (const u16*)d_in[8];
    a.v = (const u16*)d_in[9]; a.shk = (const u16*)d_in[10]; a.ssk = (const u16*)d_in[11]; a.ssd = (const u16*)d_in[12];
    a.gw = (const u16*)d_in[13]; a.out = (u16*)d_out; a.ws = (char*)d_ws;
    (void)hipFuncSetAttribute((const void*)mix_kernel<0>, hipFuncAttributeMaxDynamicSharedMemorySize, LDS_BYTES);
    mix_kernel<0><<<256, 512, LDS_BYTES, stream>>>(a);
}
```

```cpp
#include <hip/hip_runtime.h>
#include <stdint.h>

typedef unsigned short u16;
typedef unsigned int u32;
typedef unsigned long long u64;
typedef float f32x4 __attribute__((ext_vector_type(4)));
typedef float f32x2 __attribute__((ext_vector_type(2)));
typedef u32 u32x4 __attribute__((ext_vector_type(4)));
typedef u32 u32x2 __attribute__((ext_vector_type(2)));
typedef __bf16 bf16x8 __attribute__((ext_vector_type(8)));
typedef __bf16 bf16x2 __attribute__((ext_vector_type(2)));
#define GAS __attribute__((address_space(1)))
#define LAS __attribute__((address_space(3)))

constexpr int SEQ = 2048, DCH = 1024;
constexpr int US = 4624;
constexpr int U_OFF = 0;
constexpr int XY_OFF = 8 * US;
constexpr int XROW = 544;
constexpr int XCH = 53 * XROW;
constexpr int P_OFF = XY_OFF + 4 * XCH;
constexpr int PS = 644;
constexpr int R_OFF = P_OFF + 4 * PS * 4;
constexpr int LDS_BYTES = R_OFF + 128;
constexpr int KO = 4160;
constexpr int KV = 8320;
constexpr int TS = 136;
constexpr size_t WS_FLAG = 4096, WS_G = 8192, WS_PAY = 65536;

struct Args {
    const u16 *u, *lar, *aim, *cr, *ci, *ldt, *dsk, *k, *v, *shk, *ssk, *ssd, *gw;
    u16* out;
    char* ws;
};

__device__ __forceinline__ float bf2f(u32 h) { return __uint_as_float(h << 16); }
__device__ __forceinline__ float lo2f(u32 d) { return __uint_as_float(d << 16); }
__device__ __forceinline__ float hi2f(u32 d) { return __uint_as_float(d & 0xffff0000u); }
__device__ __forceinline__ u32 pk2(float a, float b) {
    bf16x2 r = {(__bf16)a, (__bf16)b};
    return __builtin_bit_cast(u32, r);
}
__device__ __forceinline__ float rbf(float a) { return lo2f(pk2(a, 0.f)); }
template <int C, int RM>
__device__ __forceinline__ float dpp_add(float v) {
    return v + __int_as_float(__builtin_amdgcn_update_dpp(0, __float_as_int(v), C, RM, 0xf, true));
}
__device__ __forceinline__ float wave_sum63(float v) {
    v = dpp_add<0x111, 0xf>(v); v = dpp_add<0x112, 0xf>(v); v = dpp_add<0x114, 0xf>(v); v = dpp_add<0x118, 0xf>(v);
    v = dpp_add<0x142, 0xa>(v); v = dpp_add<0x143, 0xc>(v);
    return v;
}
__device__ __forceinline__ f32x2 cmul(f32x2 a, f32x2 b) {
    f32x2 r;
    asm("v_pk_mul_f32 %0, %1, %2 op_sel:[0,0] op_sel_hi:[0,1]" : "=v"(r) : "v"(a), "v"(b));
    asm("v_pk_fma_f32 %0, %1, %2, %0 op_sel:[1,1,0] op_sel_hi:[1,0,1] neg_lo:[0,1,0]" : "+v"(r) : "v"(a), "v"(b));
    return r;
}
__device__ __forceinline__ f32x2 cexp(float ar, float ai, float p) {
    float m = __expf(ar * p);
    float rev = (ai * p) * 0.15915494309189535f;
    rev = __builtin_amdgcn_fractf(rev);
    f32x2 r; r.x = m * __builtin_amdgcn_cosf(rev); r.y = m * __builtin_amdgcn_sinf(rev); return r;
}
#define RLX __ATOMIC_RELAXED, __HIP_MEMORY_SCOPE_AGENT
constexpr size_t WS_PROF = 32u << 20;
#define STAMP(i)

#define AQ
#define NDS 3
template <int MODE>
__global__ void __launch_bounds__(512, 2) mix_kernel(Args p) {
    extern __shared__ __attribute__((aligned(16))) char lds[];
    const int tid = threadIdx.x, lane = tid & 63, wave = __builtin_amdgcn_readfirstlane(tid >> 6);
    const int blk = blockIdx.x;
    const int cg = (blk & 7) * 32 + (blk >> 3);
    const __amdgpu_buffer_rsrc_t wsr = __builtin_amdgcn_make_buffer_rsrc((void*)p.ws, 0, 0x7fffffff, 0x00020000);
    STAMP(0);

    const int gch = tid >> 7, gn = tid & 127;
    const int ph = tid >> 8, pc = tid & 255;
    u32x2 pu[4], pv[4], pk[7], pg; u32x4 pw0, pw1;
    u32 q_lar, q_aim, q_cr, q_ci, q_ldt;
    if (MODE != 2) {
        const int t0 = 8 * blk + 4 * ph;
        const GAS u32x2* U = (const GAS u32x2*)p.u; const GAS u32x2* V = (const GAS u32x2*)p.v; const GAS u32x2* K = (const GAS u32x2*)p.k;
#pragma unroll
        for (int i = 0; i < 7; ++i) {
            const int t = t0 - 3 + i;
            pk[i] = K[(t < 0 ? 0 : t) * 256 + pc];
        }
#pragma unroll
        for (int i = 0; i < 4; ++i) { pv[i] = V[(t0 + i) * 256 + pc]; pu[i] = U[(t0 + i) * 256 + pc]; }
        pw0 = ((const GAS u32x4*)p.shk)[2 * pc]; pw1 = ((const GAS u32x4*)p.shk)[2 * pc + 1];
        pg = ((const GAS u32x2*)p.gw)[pc];
    }
    __builtin_amdgcn_sched_barrier(0);
    STAMP(1);
    u32x4 kb[2]; u32 kbn[2]; u32 q_sk[2];
    if (MODE != 1) {
        const int o = (4 * cg + gch) * 128 + gn;
        q_lar = p.lar[o]; q_aim = p.aim[o]; q_cr = p.cr[o]; q_ci = p.ci[o]; q_ldt = p.ldt[4 * cg + gch];
    }
    const u32 q_dsk0 = p.dsk[4 * cg + (wave >> 1)];
    u64 tag = 0;
    if (MODE == 0) tag = __hip_atomic_load((GAS u64*)p.ws, RLX) + 1;
    __builtin_amdgcn_sched_barrier(0);
    if (MODE != 2) {
        if (blk == 0 && ph == 0) { pk[0] = u32x2{0u, 0u}; pk[1] = pk[0]; pk[2] = pk[0]; }
        f32x2 wa[4], wb[4];
        wa[0] = f32x2{lo2f(pw0.x), lo2f(pw0.z)}; wa[1] = f32x2{hi2f(pw0.x), hi2f(pw0.z)};
        wa[2] = f32x2{lo2f(pw0.y), lo2f(pw0.w)}; wa[3] = f32x2{hi2f(pw0.y), hi2f(pw0.w)};
        wb[0] = f32x2{lo2f(pw1.x), lo2f(pw1.z)}; wb[1] = f32x2{hi2f(pw1.x), hi2f(pw1.z)};
        wb[2] = f32x2{lo2f(pw1.y), lo2f(pw1.w)}; wb[3] = f32x2{hi2f(pw1.y), hi2f(pw1.w)};
        f32x2 ka[7], kc[7], kva[4], kvc[4]; float gs[4];
#pragma unroll
        for (int i = 0; i < 7; ++i) { ka[i] = f32x2{lo2f(pk[i].x), hi2f(pk[i].x)}; kc[i] = f32x2{lo2f(pk[i].y), hi2f(pk[i].y)}; }
        const f32x2 ga = {lo2f(pg.x), hi2f(pg.x)}, gc = {lo2f(pg.y), hi2f(pg.y)};
#pragma unroll
        for (int i = 0; i < 4; ++i) {
            const f32x2 va = {lo2f(pv[i].x), hi2f(pv[i].x)}, vc = {lo2f(pv[i].y), hi2f(pv[i].y)};
            kva[i] = (ka[i] * wa[3] + ka[i + 1] * wa[2] + ka[i + 2] * wa[1] + ka[i + 3] * wa[0]) * va;
            kvc[i] = (kc[i] * wb[3] + kc[i + 1] * wb[2] + kc[i + 2] * wb[1] + kc[i + 3] * wb[0]) * vc;
            const f32x2 ua = {lo2f(pu[i].x), hi2f(pu[i].x)}, uc = {lo2f(pu[i].y), hi2f(pu[i].y)};
            const f32x2 m = ua * ga + uc * gc;
            gs[i] = m.x + m.y;
        }
        LAS char* tb = (LAS char*)(lds + U_OFF) + pc * TS + (1 - ph) * 8;
        constexpr u32 LO = 0x05040100u, HI = 0x07060302u;
        *(LAS u32x2*)(tb + 0) = u32x2{__builtin_amdgcn_perm(pu[2].x, pu[3].x, LO), __builtin_amdgcn_perm(pu[0].x, pu[1].x, LO)};
        *(LAS u32x2*)(tb + 16) = u32x2{__builtin_amdgcn_perm(pu[2].x, pu[3].x, HI), __builtin_amdgcn_perm(pu[0].x, pu[1].x, HI)};
        *(LAS u32x2*)(tb + 32) = u32x2{__builtin_amdgcn_perm(pu[2].y, pu[3].y, LO), __builtin_amdgcn_perm(pu[0].y, pu[1].y, LO)};
        *(LAS u32x2*)(tb + 48) = u32x2{__builtin_amdgcn_perm(pu[2].y, pu[3].y, HI), __builtin_amdgcn_perm(pu[0].y, pu[1].y, HI)};
        *(LAS u32x2*)(tb + 64) = u32x2{pk2(kva[3].x, kva[2].x), pk2(kva[1].x, kva[0].x)};
        *(LAS u32x2*)(tb + 80) = u32x2{pk2(kva[3].y, kva[2].y), pk2(kva[1].y, kva[0].y)};
        *(LAS u32x2*)(tb + 96) = u32x2{pk2(kvc[3].x, kvc[2].x), pk2(kvc[1].x, kvc[0].x)};
        *(LAS u32x2*)(tb + 112) = u32x2{pk2(kvc[3].y, kvc[2].y), pk2(kvc[1].y, kvc[0].y)};
#pragma unroll
        for (int i = 0; i < 4; ++i) gs[i] = dpp_add<0x111, 0xf>(gs[i]);
#pragma unroll
        for (int i = 0; i < 4; ++i) gs[i] = dpp_add<0x112, 0xf>(gs[i]);
#pragma unroll
        for (int i = 0; i < 4; ++i) gs[i] = dpp_add<0x114, 0xf>(gs[i]);
#pragma unroll
        for (int i = 0; i < 4; ++i) gs[i] = dpp_add<0x118, 0xf>(gs[i]);
#pragma unroll
        for (int i = 0; i < 4; ++i) gs[i] = dpp_add<0x142, 0xa>(gs[i]);
#pragma unroll
        for (int i = 0; i < 4; ++i) gs[i] = dpp_add<0x143, 0xc>(gs[i]);
        if (lane == 63) { f32x4 s = {gs[0], gs[1], gs[2], gs[3]}; *(LAS f32x4*)(lds + R_OFF + wave * 16) = s; }
        STAMP(2);
        __syncthreads();
        STAMP(3);
#pragma unroll
        for (int i = 0; i < 4; ++i) {
            const int P = i * 512 + tid, c = P >> 3, q = P & 7;
            const LAS u32x2* s = (const LAS u32x2*)(lds + U_OFF + c * TS + q * 16);
            const u32x2 a = s[0], b = s[1];
            u32x4 d = {a.x, a.y, b.x, b.y};
            __builtin_amdgcn_raw_buffer_store_b128(d, wsr, (int)WS_PAY + c * 32768 + blk * 128 + q * 16, 0, 16);
        }
        if (wave == 7) {
            const int r = lane & 7, hh = r >> 2, ii = r & 3;
            const LAS float* s = (const LAS float*)(lds + R_OFF) + hh * 16 + ii;
            const float sum = rbf((s[0] + s[4]) + (s[8] + s[12]));
            const float g = __builtin_amdgcn_rcpf(1.f + __expf(-sum));
            const u32 gb = pk2(g, 0.f) & 0xffffu;
            u32x4 d;
            d.x = __builtin_amdgcn_readlane(gb, 0) | (__builtin_amdgcn_readlane(gb, 1) << 16);
            d.y = __builtin_amdgcn_readlane(gb, 2) | (__builtin_amdgcn_readlane(gb, 3) << 16);
            d.z = __builtin_amdgcn_readlane(gb, 4) | (__builtin_amdgcn_readlane(gb, 5) << 16);
            d.w = __builtin_amdgcn_readlane(gb, 6) | (__builtin_amdgcn_readlane(gb, 7) << 16);
            if (lane == 0) __builtin_amdgcn_raw_buffer_store_b128(d, wsr, (int)WS_G + blk * 16, 0, 16);
        }
    }
    STAMP(4);
    if (MODE == 1) return;
    {
        const float dt = rbf(expf(bf2f(q_ldt)));
        const float ar = -rbf(expf(bf2f(q_lar))) * dt, ai = bf2f(q_aim) * dt;
        const f32x2 X0 = {2.f * (bf2f(q_cr) * dt), 2.f * (bf2f(q_ci) * dt)};
        LAS u32* xb = (LAS u32*)(lds + XY_OFF + gch * XCH) + gn;
        constexpr int RW = XROW / 4;
        const f32x2 z = cexp(ar, ai, 1.f), w = cexp(ar, ai, 8.f), w32 = cexp(ar, ai, 256.f), e1024 = cexp(ar, ai, 1024.f);
        f32x2 X[8]; X[0] = X0;
#pragma unroll
        for (int r = 1; r < 8; ++r) X[r] = cmul(X[r - 1], z);
#pragma unroll
        for (int r = 0; r < 8; ++r) xb[r * RW] = pk2(X[r].x, X[r].y);
#pragma unroll
        for (int r = 0; r < 4; ++r) { f32x2 t = cmul(X[2 * r], w32); xb[(8 + r) * RW] = pk2(t.x, t.y); }
        const f32x2 e512 = cmul(w32, w32), e768 = cmul(e512, w32);
        { f32x2 t = cmul(X[0], e512); xb[12 * RW] = pk2(t.x, t.y); t = cmul(X[4], e512); xb[13 * RW] = pk2(t.x, t.y);
          t = cmul(X[0], e768); xb[14 * RW] = pk2(t.x, t.y); t = cmul(X[4], e768); xb[15 * RW] = pk2(t.x, t.y); }
        { const f32x2 x16 = cmul(X[0], e1024); xb[16 * RW] = pk2(x16.x, x16.y);
          f32x2 t = cmul(x16, w32); xb[17 * RW] = pk2(t.x, t.y);
          t = cmul(x16, e512); xb[18 * RW] = pk2(t.x, t.y);
          t = cmul(x16, e768); xb[19 * RW] = pk2(t.x, t.y);
          t = cmul(x16, e1024); xb[20 * RW] = pk2(t.x, t.y); }
        const f32x2 wc = {w.x, -w.y};
        const f32x2 w2 = cmul(wc, wc);
        f32x2 y0 = {1.f, 0.f}, y1 = wc;
#pragma unroll
        for (int j = 0; j < 32; j += 2) {
            xb[(21 + j) * RW] = pk2(y0.x, y0.y); xb[(22 + j) * RW] = pk2(y1.x, y1.y);
            if (j < 30) { y0 = cmul(y0, w2); y1 = cmul(y1, w2); }
        }
    }
    STAMP(5);
    if (MODE == 0) asm volatile("s_waitcnt vmcnt(0)" ::: "memory");
    __syncthreads();
    if (MODE == 0 && tid == 0) __hip_atomic_store((GAS u64*)(p.ws + WS_FLAG) + blk, tag, RLX);
    __builtin_amdgcn_sched_barrier(0);
#pragma unroll
    for (int i = 0; i < 2; ++i) {
        const int it = i * 512 + tid, ch = it >> 8, q = it & 255;
        const GAS u32* s = (const GAS u32*)(p.ssk + (4 * cg + ch) * SEQ) + 4 * q;
        kb[i] = *(const GAS u32x4*)s;
        kbn[i] = s[q < 255 ? 4 : 0];
        q_sk[i] = p.ssd[4 * cg + ch];
    }
    __builtin_amdgcn_sched_barrier(0);
    STAMP(6);
    {
        const int ch = wave >> 1, ct = wave & 1;
        const LAS char* xb = (const LAS char*)(lds + XY_OFF + ch * XCH);
        const LAS char* a0 = xb + (lane & 15) * XROW + (lane >> 4) * 16;
        const LAS char* b0 = xb + (21 + ct * 16 + (lane & 15)) * XROW + (lane >> 4) * 16;
        f32x4 d0 = {0.f, 0.f, 0.f, 0.f}, d1 = d0;
#pragma unroll
        for (int ks = 0; ks < 8; ++ks) {
            const bf16x8 fa0 = *(const LAS bf16x8*)(a0 + ks * 64), fa1 = *(const LAS bf16x8*)(a0 + 16 * XROW + ks * 64);
            const bf16x8 fb = *(const LAS bf16x8*)(b0 + ks * 64);
            d0 = __builtin_amdgcn_mfma_f32_16x16x32_bf16(fa0, fb, d0, 0, 0, 0);
            d1 = __builtin_amdgcn_mfma_f32_16x16x32_bf16(fa1, fb, d1, 0, 0, 0);
        }
        LAS float* P = (LAS float*)(lds + P_OFF) + ch * PS;
        const int j = ct * 16 + (lane & 15), rg = lane >> 4;
        if (lane == 0 && ct == 0) d0[0] += bf2f(q_dsk0);
        if (rg < 2) {
#pragma unroll
            for (int i = 0; i < 4; ++i) P[4 * rg + i + 8 * j] = d0[i];
        } else if (rg == 2) {
#pragma unroll
            for (int i = 0; i < 4; ++i) P[256 + i + 4 * j] = d0[i];
        } else {
            P[384 + 2 * j] = d0[0]; P[385 + 2 * j] = d0[1]; P[448 + 2 * j] = d0[2]; P[449 + 2 * j] = d0[3];
        }
        if (rg == 0) {
#pragma unroll
            for (int i = 0; i < 4; ++i) P[512 + 32 * i + j] = d1[i];
        } else if (rg == 1 && j == 0) P[640] = d1[0];
    }
    __syncthreads();
    STAMP(7);
#pragma unroll
    for (int i = 0; i < 2; ++i) {
        const int it = i * 512 + tid, ch = it >> 8, q = it & 255;
        const LAS float* P = (const LAS float*)(lds + P_OFF) + ch * PS;
        float k[9];
        if (q < 32) {
#pragma unroll
            for (int e = 0; e < 9; ++e) k[e] = P[8 * q + e];
        } else if (q < 64) {
            const LAS float* s = P + 256 + 4 * (q - 32);
            const float v0 = s[0], v1 = s[1], v2 = s[2], v3 = s[3], v4 = s[4];
            k[0] = v0; k[1] = v0; k[2] = v1; k[3] = v2; k[4] = v2; k[5] = v2; k[6] = v3; k[7] = v4; k[8] = v4;
        } else if (q < 128) {
            const LAS float* s = P + 384 + 2 * (q - 64);
            const float v0 = s[0], v1 = s[1], v2 = s[2];
            k[0] = v0; k[1] = v0; k[2] = v0; k[3] = v1; k[4] = v1; k[5] = v1; k[6] = v2; k[7] = v2; k[8] = v2;
        } else {
            const LAS float* s = P + 512 + (q - 128);
            const float v0 = s[0], v1 = s[1];
            k[0] = v0; k[1] = v0; k[2] = v0; k[3] = v0; k[4] = (q & 1) ? v1 : v0; k[5] = v1; k[6] = v1; k[7] = v1; k[8] = v1;
        }
        LAS char* e = (LAS char*)(lds + XY_OFF + ch * KV) + 32 + q * 16;
        u32x4 de = {pk2(k[0], k[1]), pk2(k[2], k[3]), pk2(k[4], k[5]), pk2(k[6], k[7])};
        u32x4 dz = {pk2(k[1], k[2]), pk2(k[3], k[4]), pk2(k[5], k[6]), pk2(k[7], k[8])};
        *(LAS u32x4*)e = de; *(LAS u32x4*)(e + KO) = dz;
        if (q == 0) {
            const u32x4 z = {0u, 0u, 0u, 0u};
            u32x4 ha = {0u, 0u, 0u, pk2(0.f, k[0])};
            *(LAS u32x4*)(e - 32) = z; *(LAS u32x4*)(e - 16) = z; *(LAS u32x4*)(e + KO - 32) = z; *(LAS u32x4*)(e + KO - 16) = ha;
        }
    }
    STAMP(8);
#pragma unroll
    for (int i = 0; i < 2; ++i) {
        const int it = i * 512 + tid, ch = it >> 8, q = it & 255;
        LAS char* f = (LAS char*)(lds + XY_OFF + (4 + ch) * KV) + 32 + q * 16;
        if (q == 0) kb[i].x = (kb[i].x & 0xffff0000u) | (pk2(lo2f(kb[i].x) + bf2f(q_sk[i]), 0.f) & 0xffffu);
        u32x4 dy = {__builtin_amdgcn_alignbit(kb[i].y, kb[i].x, 16), __builtin_amdgcn_alignbit(kb[i].z, kb[i].y, 16),
                    __builtin_amdgcn_alignbit(kb[i].w, kb[i].z, 16), __builtin_amdgcn_alignbit(kbn[i], kb[i].w, 16)};
        *(LAS u32x4*)f = kb[i]; *(LAS u32x4*)(f + KO) = dy;
        if (q == 0) {
            const u32x4 z = {0u, 0u, 0u, 0u};
            u32x4 hb = {0u, 0u, 0u, kb[i].x << 16};
            *(LAS u32x4*)(f - 32) = z; *(LAS u32x4*)(f - 16) = z; *(LAS u32x4*)(f + KO - 32) = z; *(LAS u32x4*)(f + KO - 16) = hb;
        }
    }
    if (tid < 240) { const u32x4 z = {0u, 0u, 0u, 0u}; *(LAS u32x4*)(lds + U_OFF + (tid / 30) * US + 4096 + (tid % 30) * 16) = z; }
    if (MODE == 0) {
        if (wave == 0) {
            const GAS u64* fl = (const GAS u64*)(p.ws + WS_FLAG);
            for (u32 spins = 0; spins < (1u << 20); ++spins) {
                const u64 f0 = __hip_atomic_load(fl + lane, RLX), f1 = __hip_atomic_load(fl + 64 + lane, RLX);
                const u64 f2 = __hip_atomic_load(fl + 128 + lane, RLX), f3 = __hip_atomic_load(fl + 192 + lane, RLX);
                if (__all(f0 == tag && f1 == tag && f2 == tag && f3 == tag)) break;
                __builtin_amdgcn_s_sleep(2);
            }
            if (blk == 0 && lane == 0) __hip_atomic_store((GAS u64*)p.ws, tag, RLX);
        }
        __syncthreads();
    }
    STAMP(9);
    u32x2 gq;
    {
        u32x4 d[4];
#pragma unroll
        for (int i = 0; i < 4; ++i) d[i] = __builtin_amdgcn_raw_buffer_load_b128(wsr, (int)WS_PAY + cg * 32768 + (i * 512 + tid) * 16, 0, 16);
        gq = __builtin_amdgcn_raw_buffer_load_b64(wsr, (int)WS_G + tid * 8, 0, 16);
#pragma unroll
        for (int i = 0; i < 4; ++i) {
            const int P = i * 512 + tid, b = P >> 3, q = P & 7;
            *(LAS u32x4*)(lds + U_OFF + q * US + 4080 - 16 * b) = d[i];
        }
    }
    __syncthreads();
    STAMP(10);
    f32x4 acc[8];
#pragma unroll
    for (int c = 0; c < 8; ++c) acc[c] = f32x4{0.f, 0.f, 0.f, 0.f};
    {
        const int j = lane & 15, kg = lane >> 4;
        const LAS char* ab = (const LAS char*)(lds + XY_OFF + wave * KV) + ((j & 1) ? 2 * (1 + j + 8 * kg) : KO + 2 * (j + 8 * kg));
        const LAS char* bb = (const LAS char*)(lds + U_OFF + wave * US) + 2 * (2032 - 16 * j + 8 * kg);
        bf16x8 A[2][8], B[2][8];
        auto ld = [&](int r, bf16x8* a_, bf16x8* b_) {
#pragma unroll
            for (int a = 0; a < 8; ++a) {
                const AQ LAS u32* s = (const AQ LAS u32*)(ab + 512 * a + 64 * r);
                u32x4 t = {s[0], s[1], s[2], s[3]};
                a_[a] = __builtin_bit_cast(bf16x8, t);
                b_[a] = *(const LAS bf16x8*)(bb - 512 * a + 64 * r);
            }
        };
        ld(0, A[0], B[0]);
#pragma unroll
        for (int r = 0; r < 8; ++r) {
            if (r < 7) ld(r + 1, A[(r + 1) & 1], B[(r + 1) & 1]);
#pragma unroll
            for (int b = 0; b < 8; ++b)
#pragma unroll
                for (int a = 0; a + b < 8; ++a)
                    acc[a + b] = __builtin_amdgcn_mfma_f32_16x16x32_bf16(A[r & 1][a], B[r & 1][b], acc[a + b], 0, 0, 0);
            if (r < 7) {
#pragma unroll
                for (int i = 0; i < 8; ++i) {
                    __builtin_amdgcn_sched_group_barrier(0x008, 2, 0);
                    __builtin_amdgcn_sched_group_barrier(0x100, NDS, 0);
                    __builtin_amdgcn_sched_group_barrier(0x008, 2, 0);
                }
                __builtin_amdgcn_sched_group_barrier(0x008, 4, 0);
            }
            __builtin_amdgcn_sched_barrier(0);
        }
    }
    STAMP(11);
    {
        const int n = lane & 15, kg = lane >> 4;
        const int sg = 4 * n + kg, ps = sg ^ ((sg >> 3) & 7);
        LAS char* rb = (LAS char*)(lds + XY_OFF + wave * KV) + ps * 16;
#pragma unroll
        for (int c = 0; c < 8; ++c) *(LAS f32x4*)(rb + c * 1024) = acc[c];
    }
    __syncthreads();
    STAMP(12);
    {
        const int ps = lane ^ ((lane >> 3) & 7);
        const LAS char* rb = (const LAS char*)(lds + XY_OFF) + wave * 1024 + ps * 16;
        const LAS char* ub = (const LAS char*)(lds + U_OFF) + 2 * (2044 - 4 * tid);
        const f32x2 g01 = {lo2f(gq.x), hi2f(gq.x)}, g23 = {lo2f(gq.y), hi2f(gq.y)};
        f32x2 o01[4], o23[4];
#pragma unroll
        for (int c = 0; c < 4; ++c) {
            const f32x4 a = *(const LAS f32x4*)(rb + c * KV), b = *(const LAS f32x4*)(rb + (4 + c) * KV);
            const u32x2 uu = *(const LAS u32x2*)(ub + c * US);
            const f32x2 u01 = {hi2f(uu.y), lo2f(uu.y)}, u23 = {hi2f(uu.x), lo2f(uu.x)};
            const f32x2 b01 = u01 * f32x2{b.x, b.y}, b23 = u23 * f32x2{b.z, b.w};
            o01[c] = g01 * (f32x2{a.x, a.y} - b01) + b01;
            o23[c] = g23 * (f32x2{a.z, a.w} - b23) + b23;
        }
        GAS char* ob = (GAS char*)(p.out + (size_t)(4 * tid) * DCH + 4 * cg);
        *(GAS u32x2*)(ob) = u32x2{pk2(o01[0].x, o01[1].x), pk2(o01[2].x, o01[3].x)};
        *(GAS u32x2*)(ob + 2 * DCH) = u32x2{pk2(o01[0].y, o01[1].y), pk2(o01[2].y, o01[3].y)};
        *(GAS u32x2*)(ob + 4 * DCH) = u32x2{pk2(o23[0].x, o23[1].x), pk2(o23[2].x, o23[3].x)};
        *(GAS u32x2*)(ob + 6 * DCH) = u32x2{pk2(o23[0].y, o23[1].y), pk2(o23[2].y, o23[3].y)};
    }
    STAMP(13);
}

extern "C" void kernel_launch(void* const* d_in, const int* in_sizes, int n_in, void* d_out, int out_size, void* d_ws,
                              size_t ws_size, hipStream_t stream) {
    Args a;
    a.u = (const u16*)d_in[0]; a.lar = (const u16*)d_in[1]; a.aim = (const u16*)d_in[2]; a.cr = (const u16*)d_in[3];
    a.ci = (const u16*)d_in[4]; a.ldt = (const u16*)d_in[5]; a.dsk = (const u16*)d_in[6]; a.k = (const u16*)d_in[8];
    a.v = (const u16*)d_in[9]; a.shk = (const u16*)d_in[10]; a.ssk = (const u16*)d_in[11]; a.ssd = (const u16*)d_in[12];
    a.gw = (const u16*)d_in[13]; a.out = (u16*)d_out; a.ws = (char*)d_ws;
    (void)hipFuncSetAttribute((const void*)mix_kernel<0>, hipFuncAttributeMaxDynamicSharedMemorySize, LDS_BYTES);
    mix_kernel<0><<<256, 512, LDS_BYTES, stream>>>(a);
}
```

```cpp
#define PFIRST
#define CODE_LINES 104
#include <hip/hip_runtime.h>
#include <stdint.h>

typedef unsigned short u16;
typedef unsigned int u32;
typedef unsigned long long u64;
typedef float f32x4 __attribute__((ext_vector_type(4)));
typedef float f32x2 __attribute__((ext_vector_type(2)));
typedef u32 u32x4 __attribute__((ext_vector_type(4)));
typedef u32 u32x2 __attribute__((ext_vector_type(2)));
typedef __bf16 bf16x8 __attribute__((ext_vector_type(8)));
typedef __bf16 bf16x2 __attribute__((ext_vector_type(2)));
#define GAS __attribute__((address_space(1)))
#define LAS __attribute__((address_space(3)))

constexpr int SEQ = 2048, DCH = 1024;
constexpr int US = 4624;
constexpr int U_OFF = 0;
constexpr int XY_OFF = 8 * US;
constexpr int XROW = 544;
constexpr int XCH = 53 * XROW;
constexpr int P_OFF = XY_OFF + 4 * XCH;
constexpr int PS = 644;
constexpr int R_OFF = P_OFF + 4 * PS * 4;
constexpr int LDS_BYTES = R_OFF + 128;
constexpr int KO = 4160;
constexpr int KV = 8320;
constexpr int TS = 136;
constexpr size_t WS_FLAG = 4096, WS_G = 8192, WS_PAY = 65536;

struct Args {
    const u16 *u, *lar, *aim, *cr, *ci, *ldt, *dsk, *k, *v, *shk, *ssk, *ssd, *gw;
    u16* out;
    char* ws;
};

__device__ __forceinline__ float bf2f(u32 h) { return __uint_as_float(h << 16); }
__device__ __forceinline__ float lo2f(u32 d) { return __uint_as_float(d << 16); }
__device__ __forceinline__ float hi2f(u32 d) { return __uint_as_float(d & 0xffff0000u); }
__device__ __forceinline__ u32 pk2(float a, float b) {
    bf16x2 r = {(__bf16)a, (__bf16)b};
    return __builtin_bit_cast(u32, r);
}
__device__ __forceinline__ float rbf(float a) { return lo2f(pk2(a, 0.f)); }
template <int C, int RM>
__device__ __forceinline__ float dpp_add(float v) {
    return v + __int_as_float(__builtin_amdgcn_update_dpp(0, __float_as_int(v), C, RM, 0xf, true));
}
__device__ __forceinline__ float wave_sum63(float v) {
    v = dpp_add<0x111, 0xf>(v); v = dpp_add<0x112, 0xf>(v); v = dpp_add<0x114, 0xf>(v); v = dpp_add<0x118, 0xf>(v);
    v = dpp_add<0x142, 0xa>(v); v = dpp_add<0x143, 0xc>(v);
    return v;
}
__device__ __forceinline__ f32x2 cmul(f32x2 a, f32x2 b) {
    f32x2 r;
    asm("v_pk_mul_f32 %0, %1, %2 op_sel:[0,0] op_sel_hi:[0,1]" : "=v"(r) : "v"(a), "v"(b));
    asm("v_pk_fma_f32 %0, %1, %2, %0 op_sel:[1,1,0] op_sel_hi:[1,0,1] neg_lo:[0,1,0]" : "+v"(r) : "v"(a), "v"(b));
    return r;
}
__device__ __forceinline__ f32x2 cexp(float ar, float ai, float p) {
    float m = __expf(ar * p);
    float rev = (ai * p) * 0.15915494309189535f;
    rev = __builtin_amdgcn_fractf(rev);
    f32x2 r; r.x = m * __builtin_amdgcn_cosf(rev); r.y = m * __builtin_amdgcn_sinf(rev); return r;
}
#define RLX __ATOMIC_RELAXED, __HIP_MEMORY_SCOPE_AGENT
constexpr size_t WS_PROF = 32u << 20;
#define STAMP(i)

#define AQ
#define NDS 3
template <int MODE>
__global__ void __launch_bounds__(512, 2) mix_kernel(Args p) {
    extern __shared__ __attribute__((aligned(16))) char lds[];
    const int tid = threadIdx.x, lane = tid & 63, wave = __builtin_amdgcn_readfirstlane(tid >> 6);
    const int blk = blockIdx.x;
    const int cg = (blk & 7) * 32 + (blk >> 3);
    const __amdgpu_buffer_rsrc_t wsr = __builtin_amdgcn_make_buffer_rsrc((void*)p.ws, 0, 0x7fffffff, 0x00020000);
    STAMP(0);

    const int gch = tid >> 7, gn = tid & 127;
    const int ph = tid >> 8, pc = tid & 255;
    u32x2 pu[4], pv[4], pk[7], pg; u32x4 pw0, pw1;
    u32 q_lar, q_aim, q_cr, q_ci, q_ldt;
    { const int o = (4 * cg + gch) * 128 + gn;
      q_lar = p.lar[o]; q_aim = p.aim[o]; q_cr = p.cr[o]; q_ci = p.ci[o]; q_ldt = p.ldt[4 * cg + gch]; }
    __builtin_amdgcn_sched_barrier(0);
    if (MODE != 2) {
        const int t0 = 8 * blk + 4 * ph;
        const GAS u32x2* U = (const GAS u32x2*)p.u; const GAS u32x2* V = (const GAS u32x2*)p.v; const GAS u32x2* K = (const GAS u32x2*)p.k;
#pragma unroll
        for (int i = 0; i < 7; ++i) {
            const int t = t0 - 3 + i;
            pk[i] = K[(t < 0 ? 0 : t) * 256 + pc];
        }
#pragma unroll
        for (int i = 0; i < 4; ++i) { pv[i] = V[(t0 + i) * 256 + pc]; pu[i] = U[(t0 + i) * 256 + pc]; }
        pw0 = ((const GAS u32x4*)p.shk)[2 * pc]; pw1 = ((const GAS u32x4*)p.shk)[2 * pc + 1];
        pg = ((const GAS u32x2*)p.gw)[pc];
    }
    __builtin_amdgcn_sched_barrier(0);
    STAMP(1);
    u32x4 kb[2]; u32 kbn[2]; u32 q_sk[2];
    const u32 q_dsk0 = p.dsk[4 * cg + (wave >> 1)];
    const u64 tag = __hip_atomic_load((GAS u64*)p.ws, RLX) + 1;
    u32 pf = 0;
    {
        u64 pc_; asm volatile("s_getpc_b64 %0" : "=s"(pc_));
        if (tid < CODE_LINES) pf = *(const GAS u32*)((pc_ & ~255ull) + tid * 128);
    }
    __builtin_amdgcn_sched_barrier(0);
    {
        const float dt = rbf(expf(bf2f(q_ldt)));
        const float ar = -rbf(expf(bf2f(q_lar))) * dt, ai = bf2f(q_aim) * dt;
        const f32x2 X0 = {2.f * (bf2f(q_cr) * dt), 2.f * (bf2f(q_ci) * dt)};
        LAS u32* xb = (LAS u32*)(lds + XY_OFF + gch * XCH) + gn;
        constexpr int RW = XROW / 4;
        const f32x2 z = cexp(ar, ai, 1.f), w = cexp(ar, ai, 8.f), w32 = cexp(ar, ai, 256.f), e1024 = cexp(ar, ai, 1024.f);
        f32x2 X[8]; X[0] = X0;
#pragma unroll
        for (int r = 1; r < 8; ++r) X[r] = cmul(X[r - 1], z);
#pragma unroll
        for (int r = 0; r < 8; ++r) xb[r * RW] = pk2(X[r].x, X[r].y);
#pragma unroll
        for (int r = 0; r < 4; ++r) { f32x2 t = cmul(X[2 * r], w32); xb[(8 + r) * RW] = pk2(t.x, t.y); }
        const f32x2 e512 = cmul(w32, w32), e768 = cmul(e512, w32);
        { f32x2 t = cmul(X[0], e512); xb[12 * RW] = pk2(t.x, t.y); t = cmul(X[4], e512); xb[13 * RW] = pk2(t.x, t.y);
          t = cmul(X[0], e768); xb[14 * RW] = pk2(t.x, t.y); t = cmul(X[4], e768); xb[15 * RW] = pk2(t.x, t.y); }
        { const f32x2 x16 = cmul(X[0], e1024); xb[16 * RW] = pk2(x16.x, x16.y);
          f32x2 t = cmul(x16, w32); xb[17 * RW] = pk2(t.x, t.y);
          t = cmul(x16, e512); xb[18 * RW] = pk2(t.x, t.y);
          t = cmul(x16, e768); xb[19 * RW] = pk2(t.x, t.y);
          t = cmul(x16, e1024); xb[20 * RW] = pk2(t.x, t.y); }
        const f32x2 wc = {w.x, -w.y};
        const f32x2 w2 = cmul(wc, wc);
        f32x2 y0 = {1.f, 0.f}, y1 = wc;
#pragma unroll
        for (int j = 0; j < 32; j += 2) {
            xb[(21 + j) * RW] = pk2(y0.x, y0.y); xb[(22 + j) * RW] = pk2(y1.x, y1.y);
            if (j < 30) { y0 = cmul(y0, w2); y1 = cmul(y1, w2); }
        }
    }
    __builtin_amdgcn_sched_barrier(0);
    STAMP(5);
    if (MODE != 2) {
        if (blk == 0 && ph == 0) { pk[0] = u32x2{0u, 0u}; pk[1] = pk[0]; pk[2] = pk[0]; }
        f32x2 wa[4], wb[4];
        wa[0] = f32x2{lo2f(pw0.x), lo2f(pw0.z)}; wa[1] = f32x2{hi2f(pw0.x), hi2f(pw0.z)};
        wa[2] = f32x2{lo2f(pw0.y), lo2f(pw0.w)}; wa[3] = f32x2{hi2f(pw0.y), hi2f(pw0.w)};
        wb[0] = f32x2{lo2f(pw1.x), lo2f(pw1.z)}; wb[1] = f32x2{hi2f(pw1.x), hi2f(pw1.z)};
        wb[2] = f32x2{lo2f(pw1.y), lo2f(pw1.w)}; wb[3] = f32x2{hi2f(pw1.y), hi2f(pw1.w)};
        f32x2 ka[7], kc[7], kva[4], kvc[4]; float gs[4];
#pragma unroll
        for (int i = 0; i < 7; ++i) { ka[i] = f32x2{lo2f(pk[i].x), hi2f(pk[i].x)}; kc[i] = f32x2{lo2f(pk[i].y), hi2f(pk[i].y)}; }
        const f32x2 ga = {lo2f(pg.x), hi2f(pg.x)}, gc = {lo2f(pg.y), hi2f(pg.y)};
#pragma unroll
        for (int i = 0; i < 4; ++i) {
            const f32x2 va = {lo2f(pv[i].x), hi2f(pv[i].x)}, vc = {lo2f(pv[i].y), hi2f(pv[i].y)};
            kva[i] = (ka[i] * wa[3] + ka[i + 1] * wa[2] + ka[i + 2] * wa[1] + ka[i + 3] * wa[0]) * va;
            kvc[i] = (kc[i] * wb[3] + kc[i + 1] * wb[2] + kc[i + 2] * wb[1] + kc[i + 3] * wb[0]) * vc;
            const f32x2 ua = {lo2f(pu[i].x), hi2f(pu[i].x)}, uc = {lo2f(pu[i].y), hi2f(pu[i].y)};
            const f32x2 m = ua * ga + uc * gc;
            gs[i] = m.x + m.y;
        }
        LAS char* tb = (LAS char*)(lds + U_OFF) + pc * TS + (1 - ph) * 8;
        constexpr u32 LO = 0x05040100u, HI = 0x07060302u;
        *(LAS u32x2*)(tb + 0) = u32x2{__builtin_amdgcn_perm(pu[2].x, pu[3].x, LO), __builtin_amdgcn_perm(pu[0].x, pu[1].x, LO)};
        *(LAS u32x2*)(tb + 16) = u32x2{__builtin_amdgcn_perm(pu[2].x, pu[3].x, HI), __builtin_amdgcn_perm(pu[0].x, pu[1].x, HI)};
        *(LAS u32x2*)(tb + 32) = u32x2{__builtin_amdgcn_perm(pu[2].y, pu[3].y, LO), __builtin_amdgcn_perm(pu[0].y, pu[1].y, LO)};
        *(LAS u32x2*)(tb + 48) = u32x2{__builtin_amdgcn_perm(pu[2].y, pu[3].y, HI), __builtin_amdgcn_perm(pu[0].y, pu[1].y, HI)};
        *(LAS u32x2*)(tb + 64) = u32x2{pk2(kva[3].x, kva[2].x), pk2(kva[1].x, kva[0].x)};
        *(LAS u32x2*)(tb + 80) = u32x2{pk2(kva[3].y, kva[2].y), pk2(kva[1].y, kva[0].y)};
        *(LAS u32x2*)(tb + 96) = u32x2{pk2(kvc[3].x, kvc[2].x), pk2(kvc[1].x, kvc[0].x)};
        *(LAS u32x2*)(tb + 112) = u32x2{pk2(kvc[3].y, kvc[2].y), pk2(kvc[1].y, kvc[0].y)};
#pragma unroll
        for (int i = 0; i < 4; ++i) gs[i] = dpp_add<0x111, 0xf>(gs[i]);
#pragma unroll
        for (int i = 0; i < 4; ++i) gs[i] = dpp_add<0x112, 0xf>(gs[i]);
#pragma unroll
        for (int i = 0; i < 4; ++i) gs[i] = dpp_add<0x114, 0xf>(gs[i]);
#pragma unroll
        for (int i = 0; i < 4; ++i) gs[i] = dpp_add<0x118, 0xf>(gs[i]);
#pragma unroll
        for (int i = 0; i < 4; ++i) gs[i] = dpp_add<0x142, 0xa>(gs[i]);
#pragma unroll
        for (int i = 0; i < 4; ++i) gs[i] = dpp_add<0x143, 0xc>(gs[i]);
        if (lane == 63) { f32x4 s = {gs[0], gs[1], gs[2], gs[3]}; *(LAS f32x4*)(lds + R_OFF + wave * 16) = s; }
        STAMP(2);
        __syncthreads();
        STAMP(3);
#pragma unroll
        for (int i = 0; i < 4; ++i) {
            const int P = i * 512 + tid, c = P >> 3, q = P & 7;
            const LAS u32x2* s = (const LAS u32x2*)(lds + U_OFF + c * TS + q * 16);
            const u32x2 a = s[0], b = s[1];
            u32x4 d = {a.x, a.y, b.x, b.y};
            __builtin_amdgcn_raw_buffer_store_b128(d, wsr, (int)WS_PAY + c * 32768 + blk * 128 + q * 16, 0, 16);
        }
        if (wave == 7) {
            const int r = lane & 7, hh = r >> 2, ii = r & 3;
            const LAS float* s = (const LAS float*)(lds + R_OFF) + hh * 16 + ii;
            const float sum = rbf((s[0] + s[4]) + (s[8] + s[12]));
            const float g = __builtin_amdgcn_rcpf(1.f + __expf(-sum));
            const u32 gb = pk2(g, 0.f) & 0xffffu;
            u32x4 d;
            d.x = __builtin_amdgcn_readlane(gb, 0) | (__builtin_amdgcn_readlane(gb, 1) << 16);
            d.y = __builtin_amdgcn_readlane(gb, 2) | (__builtin_amdgcn_readlane(gb, 3) << 16);
            d.z = __builtin_amdgcn_readlane(gb, 4) | (__builtin_amdgcn_readlane(gb, 5) << 16);
            d.w = __builtin_amdgcn_readlane(gb, 6) | (__builtin_amdgcn_readlane(gb, 7) << 16);
            if (lane == 0) __builtin_amdgcn_raw_buffer_store_b128(d, wsr, (int)WS_G + blk * 16, 0, 16);
        }
    }
    STAMP(4);
    if (MODE == 1) return;
    {
        const int ch = wave >> 1, ct = wave & 1;
        const LAS char* xb = (const LAS char*)(lds + XY_OFF + ch * XCH);
        const LAS char* a0 = xb + (lane & 15) * XROW + (lane >> 4) * 16;
        const LAS char* b0 = xb + (21 + ct * 16 + (lane & 15)) * XROW + (lane >> 4) * 16;
        f32x4 d0 = {0.f, 0.f, 0.f, 0.f}, d1 = d0;
#pragma unroll
        for (int ks = 0; ks < 8; ++ks) {
            const bf16x8 fa0 = *(const LAS bf16x8*)(a0 + ks * 64), fa1 = *(const LAS bf16x8*)(a0 + 16 * XROW + ks * 64);
            const bf16x8 fb = *(const LAS bf16x8*)(b0 + ks * 64);
            d0 = __builtin_amdgcn_mfma_f32_16x16x32_bf16(fa0, fb, d0, 0, 0, 0);
            d1 = __builtin_amdgcn_mfma_f32_16x16x32_bf16(fa1, fb, d1, 0, 0, 0);
        }
        LAS float* P = (LAS float*)(lds + P_OFF) + ch * PS;
        const int j = ct * 16 + (lane & 15), rg = lane >> 4;
        if (lane == 0 && ct == 0) d0[0] += bf2f(q_dsk0);
        if (rg < 2) {
#pragma unroll
            for (int i = 0; i < 4; ++i) P[4 * rg + i + 8 * j] = d0[i];
        } else if (rg == 2) {
#pragma unroll
            for (int i = 0; i < 4; ++i) P[256 + i + 4 * j] = d0[i];
        } else {
            P[384 + 2 * j] = d0[0]; P[385 + 2 * j] = d0[1]; P[448 + 2 * j] = d0[2]; P[449 + 2 * j] = d0[3];
        }
        if (rg == 0) {
#pragma unroll
            for (int i = 0; i < 4; ++i) P[512 + 32 * i + j] = d1[i];
        } else if (rg == 1 && j == 0) P[640] = d1[0];
    }
    STAMP(7);
    asm volatile("" :: "v"(pf));
    if (MODE == 0) asm volatile("s_waitcnt vmcnt(0)" ::: "memory");
    __syncthreads();
    if (MODE == 0 && tid == 0) __hip_atomic_store((GAS u64*)(p.ws + WS_FLAG) + blk, tag, RLX);
    __builtin_amdgcn_sched_barrier(0);
#pragma unroll
    for (int i = 0; i < 2; ++i) {
        const int it = i * 512 + tid, ch = it >> 8, q = it & 255;
        const GAS u32* s = (const GAS u32*)(p.ssk + (4 * cg + ch) * SEQ) + 4 * q;
        kb[i] = *(const GAS u32x4*)s;
        kbn[i] = s[q < 255 ? 4 : 0];
        q_sk[i] = p.ssd[4 * cg + ch];
    }
    __builtin_amdgcn_sched_barrier(0);
    STAMP(6);
#pragma unroll
    for (int i = 0; i < 2; ++i) {
        const int it = i * 512 + tid, ch = it >> 8, q = it & 255;
        const LAS float* P = (const LAS float*)(lds + P_OFF) + ch * PS;
        float k[9];
        if (q < 32) {
#pragma unroll
            for (int e = 0; e < 9; ++e) k[e] = P[8 * q + e];
        } else if (q < 64) {
            const LAS float* s = P + 256 + 4 * (q - 32);
            const float v0 = s[0], v1 = s[1], v2 = s[2], v3 = s[3], v4 = s[4];
            k[0] = v0; k[1] = v0; k[2] = v1; k[3] = v2; k[4] = v2; k[5] = v2; k[6] = v3; k[7] = v4; k[8] = v4;
        } else if (q < 128) {
            const LAS float* s = P + 384 + 2 * (q - 64);
            const float v0 = s[0], v1 = s[1], v2 = s[2];
            k[0] = v0; k[1] = v0; k[2] = v0; k[3] = v1; k[4] = v1; k[5] = v1; k[6] = v2; k[7] = v2; k[8] = v2;
        } else {
            const LAS float* s = P + 512 + (q - 128);
            const float v0 = s[0], v1 = s[1];
            k[0] = v0; k[1] = v0; k[2] = v0; k[3] = v0; k[4] = (q & 1) ? v1 : v0; k[5] = v1; k[6] = v1; k[7] = v1; k[8] = v1;
        }
        LAS char* e = (LAS char*)(lds + XY_OFF + ch * KV) + 32 + q * 16;
        u32x4 de = {pk2(k[0], k[1]), pk2(k[2], k[3]), pk2(k[4], k[5]), pk2(k[6], k[7])};
        u32x4 dz = {pk2(k[1], k[2]), pk2(k[3], k[4]), pk2(k[5], k[6]), pk2(k[7], k[8])};
        *(LAS u32x4*)e = de; *(LAS u32x4*)(e + KO) = dz;
        if (q == 0) {
            const u32x4 z = {0u, 0u, 0u, 0u};
            u32x4 ha = {0u, 0u, 0u, pk2(0.f, k[0])};
            *(LAS u32x4*)(e - 32) = z; *(LAS u32x4*)(e - 16) = z; *(LAS u32x4*)(e + KO - 32) = z; *(LAS u32x4*)(e + KO - 16) = ha;
        }
    }
    STAMP(8);
#pragma unroll
    for (int i = 0; i < 2; ++i) {
        const int it = i * 512 + tid, ch = it >> 8, q = it & 255;
        LAS char* f = (LAS char*)(lds + XY_OFF + (4 + ch) * KV) + 32 + q * 16;
        if (q == 0) kb[i].x = (kb[i].x & 0xffff0000u) | (pk2(lo2f(kb[i].x) + bf2f(q_sk[i]), 0.f) & 0xffffu);
        u32x4 dy = {__builtin_amdgcn_alignbit(kb[i].y, kb[i].x, 16), __builtin_amdgcn_alignbit(kb[i].z, kb[i].y, 16),
                    __builtin_amdgcn_alignbit(kb[i].w, kb[i].z, 16), __builtin_amdgcn_alignbit(kbn[i], kb[i].w, 16)};
        *(LAS u32x4*)f = kb[i]; *(LAS u32x4*)(f + KO) = dy;
        if (q == 0) {
            const u32x4 z = {0u, 0u, 0u, 0u};
            u32x4 hb = {0u, 0u, 0u, kb[i].x << 16};
            *(LAS u32x4*)(f - 32) = z; *(LAS u32x4*)(f - 16) = z; *(LAS u32x4*)(f + KO - 32) = z; *(LAS u32x4*)(f + KO - 16) = hb;
        }
    }
    if (tid < 240) { const u32x4 z = {0u, 0u, 0u, 0u}; *(LAS u32x4*)(lds + U_OFF + (tid / 30) * US + 4096 + (tid % 30) * 16) = z; }
    u32x2 gq;
    {
        const GAS u64* fl = (const GAS u64*)(p.ws + WS_FLAG) + wave * 32 + (lane & 31);
#pragma unroll 1
        for (u32 spins = 0; spins < (1u << 20); ++spins) {
            const u64 f0 = __hip_atomic_load(fl, RLX);
            if (__all(f0 == tag)) break;
            __builtin_amdgcn_s_sleep(1);
        }
        STAMP(9);
        u32x4 d[4];
#pragma unroll
        for (int i = 0; i < 4; ++i) d[i] = __builtin_amdgcn_raw_buffer_load_b128(wsr, (int)WS_PAY + cg * 32768 + wave * 4096 + (i * 64 + lane) * 16, 0, 16);
        gq = __builtin_amdgcn_raw_buffer_load_b64(wsr, (int)WS_G + tid * 8, 0, 16);
#pragma unroll
        for (int i = 0; i < 4; ++i) {
            const int b = wave * 32 + i * 8 + (lane >> 3), q = lane & 7;
            *(LAS u32x4*)(lds + U_OFF + q * US + 4080 - 16 * b) = d[i];
        }
    }
    __syncthreads();
    if (blk == 0 && tid == 0) __hip_atomic_store((GAS u64*)p.ws, tag, RLX);
    STAMP(10);
    f32x4 acc[8];
#pragma unroll
    for (int c = 0; c < 8; ++c) acc[c] = f32x4{0.f, 0.f, 0.f, 0.f};
    {
        const int j = lane & 15, kg = lane >> 4;
        const LAS char* ab = (const LAS char*)(lds + XY_OFF + wave * KV) + ((j & 1) ? 2 * (1 + j + 8 * kg) : KO + 2 * (j + 8 * kg));
        const LAS char* bb = (const LAS char*)(lds + U_OFF + wave * US) + 2 * (2032 - 16 * j + 8 * kg);
        bf16x8 A[2][8], B[2][8];
        auto ld = [&](int r, bf16x8* a_, bf16x8* b_) {
#pragma unroll
            for (int a = 0; a < 8; ++a) {
                const AQ LAS u32* s = (const AQ LAS u32*)(ab + 512 * a + 64 * r);
                u32x4 t = {s[0], s[1], s[2], s[3]};
                a_[a] = __builtin_bit_cast(bf16x8, t);
                b_[a] = *(const LAS bf16x8*)(bb - 512 * a + 64 * r);
            }
        };
        ld(0, A[0], B[0]);
#pragma unroll
        for (int r = 0; r < 8; ++r) {
            if (r < 7) ld(r + 1, A[(r + 1) & 1], B[(r + 1) & 1]);
#pragma unroll
            for (int b = 0; b < 8; ++b)
#pragma unroll
                for (int a = 0; a + b < 8; ++a)
                    acc[a + b] = __builtin_amdgcn_mfma_f32_16x16x32_bf16(A[r & 1][a], B[r & 1][b], acc[a + b], 0, 0, 0);
            if (r < 7) {
#pragma unroll
                for (int i = 0; i < 8; ++i) {
                    __builtin_amdgcn_sched_group_barrier(0x008, 2, 0);
                    __builtin_amdgcn_sched_group_barrier(0x100, NDS, 0);
                    __builtin_amdgcn_sched_group_barrier(0x008, 2, 0);
                }
                __builtin_amdgcn_sched_group_barrier(0x008, 4, 0);
            }
            __builtin_amdgcn_sched_barrier(0);
        }
    }
    STAMP(11);
    {
        const int n = lane & 15, kg = lane >> 4;
        const int sg = 4 * n + kg, ps = sg ^ ((sg >> 3) & 7);
        LAS char* rb = (LAS char*)(lds + XY_OFF + wave * KV) + ps * 16;
#pragma unroll
        for (int c = 0; c < 8; ++c) *(LAS f32x4*)(rb + c * 1024) = acc[c];
    }
    __syncthreads();
    STAMP(12);
    {
        const int ps = lane ^ ((lane >> 3) & 7);
        const LAS char* rb = (const LAS char*)(lds + XY_OFF) + wave * 1024 + ps * 16;
        const LAS char* ub = (const LAS char*)(lds + U_OFF) + 2 * (2044 - 4 * tid);
        const f32x2 g01 = {lo2f(gq.x), hi2f(gq.x)}, g23 = {lo2f(gq.y), hi2f(gq.y)};
        f32x2 o01[4], o23[4];
#pragma unroll
        for (int c = 0; c < 4; ++c) {
            const f32x4 a = *(const LAS f32x4*)(rb + c * KV), b = *(const LAS f32x4*)(rb + (4 + c) * KV);
            const u32x2 uu = *(const LAS u32x2*)(ub + c * US);
            const f32x2 u01 = {hi2f(uu.y), lo2f(uu.y)}, u23 = {hi2f(uu.x), lo2f(uu.x)};
            const f32x2 b01 = u01 * f32x2{b.x, b.y}, b23 = u23 * f32x2{b.z, b.w};
            o01[c] = g01 * (f32x2{a.x, a.y} - b01) + b01;
            o23[c] = g23 * (f32x2{a.z, a.w} - b23) + b23;
        }
        GAS char* ob = (GAS char*)(p.out + (size_t)(4 * tid) * DCH + 4 * cg);
        *(GAS u32x2*)(ob) = u32x2{pk2(o01[0].x, o01[1].x), pk2(o01[2].x, o01[3].x)};
        *(GAS u32x2*)(ob + 2 * DCH) = u32x2{pk2(o01[0].y, o01[1].y), pk2(o01[2].y, o01[3].y)};
        *(GAS u32x2*)(ob + 4 * DCH) = u32x2{pk2(o23[0].x, o23[1].x), pk2(o23[2].x, o23[3].x)};
        *(GAS u32x2*)(ob + 6 * DCH) = u32x2{pk2(o23[0].y, o23[1].y), pk2(o23[2].y, o23[3].y)};
    }
    STAMP(13);
}

extern "C" void kernel_launch(void* const* d_in, const int* in_sizes, int n_in, void* d_out, int out_size, void* d_ws,
                              size_t ws_size, hipStream_t stream) {
    Args a;
    a.u = (const u16*)d_in[0]; a.lar = (const u16*)d_in[1]; a.aim = (const u16*)d_in[2]; a.cr = (const u16*)d_in[3];
    a.ci = (const u16*)d_in[4]; a.ldt = (const u16*)d_in[5]; a.dsk = (const u16*)d_in[6]; a.k = (const u16*)d_in[8];
    a.v = (const u16*)d_in[9]; a.shk = (const u16*)d_in[10]; a.ssk = (const u16*)d_in[11]; a.ssd = (const u16*)d_in[12];
    a.gw = (const u16*)d_in[13]; a.out = (u16*)d_out; a.ws = (char*)d_ws;
    (void)hipFuncSetAttribute((const void*)mix_kernel<0>, hipFuncAttributeMaxDynamicSharedMemorySize, LDS_BYTES);
    mix_kernel<0><<<256, 512, LDS_BYTES, stream>>>(a);
}
```

```cpp
#define PFIRST
#define CODE_LINES 100
#define PAYL 0
#include <hip/hip_runtime.h>
#include <stdint.h>

typedef unsigned short u16;
typedef unsigned int u32;
typedef unsigned long long u64;
typedef float f32x4 __attribute__((ext_vector_type(4)));
typedef float f32x2 __attribute__((ext_vector_type(2)));
typedef u32 u32x4 __attribute__((ext_vector_type(4)));
typedef u32 u32x2 __attribute__((ext_vector_type(2)));
typedef __bf16 bf16x8 __attribute__((ext_vector_type(8)));
typedef __bf16 bf16x2 __attribute__((ext_vector_type(2)));
#define GAS __attribute__((address_space(1)))
#define LAS __attribute__((address_space(3)))

constexpr int SEQ = 2048, DCH = 1024;
constexpr int US = 4624;
constexpr int U_OFF = 0;
constexpr int XY_OFF = 8 * US;
constexpr int XROW = 544;
constexpr int XCH = 53 * XROW;
constexpr int P_OFF = XY_OFF + 4 * XCH;
constexpr int PS = 644;
constexpr int R_OFF = P_OFF + 4 * PS * 4;
constexpr int LDS_BYTES = R_OFF + 128;
constexpr int KO = 4160;
constexpr int KV = 8320;
constexpr int TS = 72;
constexpr int T_OFF = 4 * US;
constexpr size_t WS_FLAG = 4096, WS_G = 8192, WS_PAY = 65536;

struct Args {
    const u16 *u, *lar, *aim, *cr, *ci, *ldt, *dsk, *k, *v, *shk, *ssk, *ssd, *gw;
    u16* out;
    char* ws;
};

__device__ __forceinline__ float bf2f(u32 h) { return __uint_as_float(h << 16); }
__device__ __forceinline__ float lo2f(u32 d) { return __uint_as_float(d << 16); }
__device__ __forceinline__ float hi2f(u32 d) { return __uint_as_float(d & 0xffff0000u); }
__device__ __forceinline__ u32 pk2(float a, float b) {
    bf16x2 r = {(__bf16)a, (__bf16)b};
    return __builtin_bit_cast(u32, r);
}
__device__ __forceinline__ float rbf(float a) { return lo2f(pk2(a, 0.f)); }
template <int C, int RM>
__device__ __forceinline__ float dpp_add(float v) {
    return v + __int_as_float(__builtin_amdgcn_update_dpp(0, __float_as_int(v), C, RM, 0xf, true));
}
__device__ __forceinline__ float wave_sum63(float v) {
    v = dpp_add<0x111, 0xf>(v); v = dpp_add<0x112, 0xf>(v); v = dpp_add<0x114, 0xf>(v); v = dpp_add<0x118, 0xf>(v);
    v = dpp_add<0x142, 0xa>(v); v = dpp_add<0x143, 0xc>(v);
    return v;
}
__device__ __forceinline__ f32x2 cmul(f32x2 a, f32x2 b) {
    f32x2 r;
    asm("v_pk_mul_f32 %0, %1, %2 op_sel:[0,0] op_sel_hi:[0,1]" : "=v"(r) : "v"(a), "v"(b));
    asm("v_pk_fma_f32 %0, %1, %2, %0 op_sel:[1,1,0] op_sel_hi:[1,0,1] neg_lo:[0,1,0]" : "+v"(r) : "v"(a), "v"(b));
    return r;
}
__device__ __forceinline__ f32x2 cexp(float ar, float ai, float p) {
    float m = __expf(ar * p);
    float rev = (ai * p) * 0.15915494309189535f;
    rev = __builtin_amdgcn_fractf(rev);
    f32x2 r; r.x = m * __builtin_amdgcn_cosf(rev); r.y = m * __builtin_amdgcn_sinf(rev); return r;
}
#define RLX __ATOMIC_RELAXED, __HIP_MEMORY_SCOPE_AGENT
constexpr size_t WS_PROF = 32u << 20;
#define STAMP(i)

#define AQ
#define NDS 3
template <int MODE>
__global__ void __launch_bounds__(512, 2) mix_kernel(Args p) {
    extern __shared__ __attribute__((aligned(16))) char lds[];
    const int tid = threadIdx.x, lane = tid & 63, wave = __builtin_amdgcn_readfirstlane(tid >> 6);
    const int blk = blockIdx.x;
    const int cg = (blk & 7) * 32 + (blk >> 3);
    const __amdgpu_buffer_rsrc_t wsr = __builtin_amdgcn_make_buffer_rsrc((void*)p.ws, 0, 0x7fffffff, 0x00020000);
    STAMP(0);

    const int gch = tid >> 7, gn = tid & 127;
    const int ph = tid >> 8, pc = tid & 255;
    u32x2 pu[4], pv[4], pk[7], pg; u32x4 pw0, pw1;
    u32 q_lar, q_aim, q_cr, q_ci, q_ldt;
    { const int o = (4 * cg + gch) * 128 + gn;
      q_lar = p.lar[o]; q_aim = p.aim[o]; q_cr = p.cr[o]; q_ci = p.ci[o]; q_ldt = p.ldt[4 * cg + gch]; }
    __builtin_amdgcn_sched_barrier(0);
    if (MODE != 2) {
        const int t0 = 8 * blk + 4 * ph;
        const GAS u32x2* U = (const GAS u32x2*)p.u; const GAS u32x2* V = (const GAS u32x2*)p.v; const GAS u32x2* K = (const GAS u32x2*)p.k;
#pragma unroll
        for (int i = 0; i < 7; ++i) {
            const int t = t0 - 3 + i;
            pk[i] = K[(t < 0 ? 0 : t) * 256 + pc];
        }
#pragma unroll
        for (int i = 0; i < 4; ++i) { pv[i] = V[(t0 + i) * 256 + pc]; pu[i] = U[(t0 + i) * 256 + pc]; }
        pw0 = ((const GAS u32x4*)p.shk)[2 * pc]; pw1 = ((const GAS u32x4*)p.shk)[2 * pc + 1];
        pg = ((const GAS u32x2*)p.gw)[pc];
    }
    __builtin_amdgcn_sched_barrier(0);
    STAMP(1);
    u32x4 kb[2]; u32 kbn[2]; u32 q_sk[2];
    const u32 q_dsk0 = p.dsk[4 * cg + (wave >> 1)];
    const u64 tag = __hip_atomic_load((GAS u64*)p.ws, RLX) + 1;
    u32 pf = 0;
    {
        u64 pc_; asm volatile("s_getpc_b64 %0" : "=s"(pc_));
        if (tid < CODE_LINES) pf = *(const GAS u32*)((pc_ & ~255ull) + tid * 128);
    }
    __builtin_amdgcn_sched_barrier(0);
    auto XYGEN = [&]() __attribute__((always_inline)) {
    {
        const float dt = rbf(expf(bf2f(q_ldt)));
        const float ar = -rbf(expf(bf2f(q_lar))) * dt, ai = bf2f(q_aim) * dt;
        const f32x2 X0 = {2.f * (bf2f(q_cr) * dt), 2.f * (bf2f(q_ci) * dt)};
        LAS u32* xb = (LAS u32*)(lds + XY_OFF + gch * XCH) + gn;
        constexpr int RW = XROW / 4;
        const f32x2 z = cexp(ar, ai, 1.f), w = cexp(ar, ai, 8.f), w32 = cexp(ar, ai, 256.f), e1024 = cexp(ar, ai, 1024.f);
        f32x2 X[8]; X[0] = X0;
#pragma unroll
        for (int r = 1; r < 8; ++r) X[r] = cmul(X[r - 1], z);
#pragma unroll
        for (int r = 0; r < 8; ++r) xb[r * RW] = pk2(X[r].x, X[r].y);
#pragma unroll
        for (int r = 0; r < 4; ++r) { f32x2 t = cmul(X[2 * r], w32); xb[(8 + r) * RW] = pk2(t.x, t.y); }
        const f32x2 e512 = cmul(w32, w32), e768 = cmul(e512, w32);
        { f32x2 t = cmul(X[0], e512); xb[12 * RW] = pk2(t.x, t.y); t = cmul(X[4], e512); xb[13 * RW] = pk2(t.x, t.y);
          t = cmul(X[0], e768); xb[14 * RW] = pk2(t.x, t.y); t = cmul(X[4], e768); xb[15 * RW] = pk2(t.x, t.y); }
        { const f32x2 x16 = cmul(X[0], e1024); xb[16 * RW] = pk2(x16.x, x16.y);
          f32x2 t = cmul(x16, w32); xb[17 * RW] = pk2(t.x, t.y);
          t = cmul(x16, e512); xb[18 * RW] = pk2(t.x, t.y);
          t = cmul(x16, e768); xb[19 * RW] = pk2(t.x, t.y);
          t = cmul(x16, e1024); xb[20 * RW] = pk2(t.x, t.y); }
        const f32x2 wc = {w.x, -w.y};
        const f32x2 w2 = cmul(wc, wc);
        f32x2 y0 = {1.f, 0.f}, y1 = wc;
#pragma unroll
        for (int j = 0; j < 32; j += 2) {
            xb[(21 + j) * RW] = pk2(y0.x, y0.y); xb[(22 + j) * RW] = pk2(y1.x, y1.y);
            if (j < 30) { y0 = cmul(y0, w2); y1 = cmul(y1, w2); }
        }
    }
    };
    XYGEN();
    __builtin_amdgcn_sched_barrier(0);
    STAMP(5);
    if (MODE != 2) {
        if (blk == 0 && ph == 0) { pk[0] = u32x2{0u, 0u}; pk[1] = pk[0]; pk[2] = pk[0]; }
        f32x2 wa[4], wb[4];
        wa[0] = f32x2{lo2f(pw0.x), lo2f(pw0.z)}; wa[1] = f32x2{hi2f(pw0.x), hi2f(pw0.z)};
        wa[2] = f32x2{lo2f(pw0.y), lo2f(pw0.w)}; wa[3] = f32x2{hi2f(pw0.y), hi2f(pw0.w)};
        wb[0] = f32x2{lo2f(pw1.x), lo2f(pw1.z)}; wb[1] = f32x2{hi2f(pw1.x), hi2f(pw1.z)};
        wb[2] = f32x2{lo2f(pw1.y), lo2f(pw1.w)}; wb[3] = f32x2{hi2f(pw1.y), hi2f(pw1.w)};
        f32x2 ka[7], kc[7], kva[4], kvc[4]; float gs[4];
#pragma unroll
        for (int i = 0; i < 7; ++i) { ka[i] = f32x2{lo2f(pk[i].x), hi2f(pk[i].x)}; kc[i] = f32x2{lo2f(pk[i].y), hi2f(pk[i].y)}; }
        const f32x2 ga = {lo2f(pg.x), hi2f(pg.x)}, gc = {lo2f(pg.y), hi2f(pg.y)};
#pragma unroll
        for (int i = 0; i < 4; ++i) {
            const f32x2 va = {lo2f(pv[i].x), hi2f(pv[i].x)}, vc = {lo2f(pv[i].y), hi2f(pv[i].y)};
            kva[i] = (ka[i] * wa[3] + ka[i + 1] * wa[2] + ka[i + 2] * wa[1] + ka[i + 3] * wa[0]) * va;
            kvc[i] = (kc[i] * wb[3] + kc[i + 1] * wb[2] + kc[i + 2] * wb[1] + kc[i + 3] * wb[0]) * vc;
            const f32x2 ua = {lo2f(pu[i].x), hi2f(pu[i].x)}, uc = {lo2f(pu[i].y), hi2f(pu[i].y)};
            const f32x2 m = ua * ga + uc * gc;
            gs[i] = m.x + m.y;
        }
        LAS char* tb = (LAS char*)(lds + T_OFF) + pc * TS + (1 - ph) * 8;
        *(LAS u32x2*)(tb + 0) = u32x2{pk2(kva[3].x, kva[2].x), pk2(kva[1].x, kva[0].x)};
        *(LAS u32x2*)(tb + 16) = u32x2{pk2(kva[3].y, kva[2].y), pk2(kva[1].y, kva[0].y)};
        *(LAS u32x2*)(tb + 32) = u32x2{pk2(kvc[3].x, kvc[2].x), pk2(kvc[1].x, kvc[0].x)};
        *(LAS u32x2*)(tb + 48) = u32x2{pk2(kvc[3].y, kvc[2].y), pk2(kvc[1].y, kvc[0].y)};
#pragma unroll
        for (int i = 0; i < 4; ++i) gs[i] = dpp_add<0x111, 0xf>(gs[i]);
#pragma unroll
        for (int i = 0; i < 4; ++i) gs[i] = dpp_add<0x112, 0xf>(gs[i]);
#pragma unroll
        for (int i = 0; i < 4; ++i) gs[i] = dpp_add<0x114, 0xf>(gs[i]);
#pragma unroll
        for (int i = 0; i < 4; ++i) gs[i] = dpp_add<0x118, 0xf>(gs[i]);
#pragma unroll
        for (int i = 0; i < 4; ++i) gs[i] = dpp_add<0x142, 0xa>(gs[i]);
#pragma unroll
        for (int i = 0; i < 4; ++i) gs[i] = dpp_add<0x143, 0xc>(gs[i]);
        if (lane == 63) { f32x4 s = {gs[0], gs[1], gs[2], gs[3]}; *(LAS f32x4*)(lds + R_OFF + wave * 16) = s; }
        STAMP(2);
        __syncthreads();
        STAMP(3);
#pragma unroll
        for (int i = 0; i < 2; ++i) {
            const int P = i * 512 + tid, c = P >> 2, q = P & 3;
            const LAS u32x2* s = (const LAS u32x2*)(lds + T_OFF + c * TS + q * 16);
            const u32x2 a = s[0], b = s[1];
            u32x4 d = {a.x, a.y, b.x, b.y};
            __builtin_amdgcn_raw_buffer_store_b128(d, wsr, (int)WS_PAY + c * 16384 + blk * 64 + q * 16, 0, 16);
        }
        if (wave == 7) {
            const int r = lane & 7, hh = r >> 2, ii = r & 3;
            const LAS float* s = (const LAS float*)(lds + R_OFF) + hh * 16 + ii;
            const float sum = rbf((s[0] + s[4]) + (s[8] + s[12]));
            const float g = __builtin_amdgcn_rcpf(1.f + __expf(-sum));
            const u32 gb = pk2(g, 0.f) & 0xffffu;
            u32x4 d;
            d.x = __builtin_amdgcn_readlane(gb, 0) | (__builtin_amdgcn_readlane(gb, 1) << 16);
            d.y = __builtin_amdgcn_readlane(gb, 2) | (__builtin_amdgcn_readlane(gb, 3) << 16);
            d.z = __builtin_amdgcn_readlane(gb, 4) | (__builtin_amdgcn_readlane(gb, 5) << 16);
            d.w = __builtin_amdgcn_readlane(gb, 6) | (__builtin_amdgcn_readlane(gb, 7) << 16);
            if (lane == 0) __builtin_amdgcn_raw_buffer_store_b128(d, wsr, (int)WS_G + blk * 16, 0, 16);
        }
    }
    STAMP(4);
    if (MODE == 1) return;
    {
        const int ch = wave >> 1, ct = wave & 1;
        const LAS char* xb = (const LAS char*)(lds + XY_OFF + ch * XCH);
        const LAS char* a0 = xb + (lane & 15) * XROW + (lane >> 4) * 16;
        const LAS char* b0 = xb + (21 + ct * 16 + (lane & 15)) * XROW + (lane >> 4) * 16;
        f32x4 d0 = {0.f, 0.f, 0.f, 0.f}, d1 = d0;
#pragma unroll
        for (int ks = 0; ks < 8; ++ks) {
            const bf16x8 fa0 = *(const LAS bf16x8*)(a0 + ks * 64), fa1 = *(const LAS bf16x8*)(a0 + 16 * XROW + ks * 64);
            const bf16x8 fb = *(const LAS bf16x8*)(b0 + ks * 64);
            d0 = __builtin_amdgcn_mfma_f32_16x16x32_bf16(fa0, fb, d0, 0, 0, 0);
            d1 = __builtin_amdgcn_mfma_f32_16x16x32_bf16(fa1, fb, d1, 0, 0, 0);
        }
        LAS float* P = (LAS float*)(lds + P_OFF) + ch * PS;
        const int j = ct * 16 + (lane & 15), rg = lane >> 4;
        if (lane == 0 && ct == 0) d0[0] += bf2f(q_dsk0);
        if (rg < 2) {
#pragma unroll
            for (int i = 0; i < 4; ++i) P[4 * rg + i + 8 * j] = d0[i];
        } else if (rg == 2) {
#pragma unroll
            for (int i = 0; i < 4; ++i) P[256 + i + 4 * j] = d0[i];
        } else {
            P[384 + 2 * j] = d0[0]; P[385 + 2 * j] = d0[1]; P[448 + 2 * j] = d0[2]; P[449 + 2 * j] = d0[3];
        }
        if (rg == 0) {
#pragma unroll
            for (int i = 0; i < 4; ++i) P[512 + 32 * i + j] = d1[i];
        } else if (rg == 1 && j == 0) P[640] = d1[0];
    }
    STAMP(7);
    asm volatile("" :: "v"(pf));
    if (MODE == 0) asm volatile("s_waitcnt vmcnt(0)" ::: "memory");
    __syncthreads();
    if (MODE == 0 && tid == 0) __hip_atomic_store((GAS u64*)(p.ws + WS_FLAG) + blk, tag, RLX);
    __builtin_amdgcn_sched_barrier(0);
#pragma unroll
    for (int i = 0; i < 2; ++i) {
        const int it = i * 512 + tid, ch = it >> 8, q = it & 255;
        const GAS u32* s = (const GAS u32*)(p.ssk + (4 * cg + ch) * SEQ) + 4 * q;
        kb[i] = *(const GAS u32x4*)s;
        kbn[i] = s[q < 255 ? 4 : 0];
        q_sk[i] = p.ssd[4 * cg + ch];
    }
    u32x2 cu[4];
#pragma unroll
    for (int i = 0; i < 4; ++i) cu[i] = ((const GAS u32x2*)p.u)[(4 * tid + i) * 256 + cg];
    __builtin_amdgcn_sched_barrier(0);
    STAMP(6);
#pragma unroll
    for (int i = 0; i < 2; ++i) {
        const int it = i * 512 + tid, ch = it >> 8, q = it & 255;
        const LAS float* P = (const LAS float*)(lds + P_OFF) + ch * PS;
        float k[9];
        if (q < 32) {
#pragma unroll
            for (int e = 0; e < 9; ++e) k[e] = P[8 * q + e];
        } else if (q < 64) {
            const LAS float* s = P + 256 + 4 * (q - 32);
            const float v0 = s[0], v1 = s[1], v2 = s[2], v3 = s[3], v4 = s[4];
            k[0] = v0; k[1] = v0; k[2] = v1; k[3] = v2; k[4] = v2; k[5] = v2; k[6] = v3; k[7] = v4; k[8] = v4;
        } else if (q < 128) {
            const LAS float* s = P + 384 + 2 * (q - 64);
            const float v0 = s[0], v1 = s[1], v2 = s[2];
            k[0] = v0; k[1] = v0; k[2] = v0; k[3] = v1; k[4] = v1; k[5] = v1; k[6] = v2; k[7] = v2; k[8] = v2;
        } else {
            const LAS float* s = P + 512 + (q - 128);
            const float v0 = s[0], v1 = s[1];
            k[0] = v0; k[1] = v0; k[2] = v0; k[3] = v0; k[4] = (q & 1) ? v1 : v0; k[5] = v1; k[6] = v1; k[7] = v1; k[8] = v1;
        }
        LAS char* e = (LAS char*)(lds + XY_OFF + ch * KV) + 32 + q * 16;
        u32x4 de = {pk2(k[0], k[1]), pk2(k[2], k[3]), pk2(k[4], k[5]), pk2(k[6], k[7])};
        u32x4 dz = {pk2(k[1], k[2]), pk2(k[3], k[4]), pk2(k[5], k[6]), pk2(k[7], k[8])};
        *(LAS u32x4*)e = de; *(LAS u32x4*)(e + KO) = dz;
        if (q == 0) {
            const u32x4 z = {0u, 0u, 0u, 0u};
            u32x4 ha = {0u, 0u, 0u, pk2(0.f, k[0])};
            *(LAS u32x4*)(e - 32) = z; *(LAS u32x4*)(e - 16) = z; *(LAS u32x4*)(e + KO - 32) = z; *(LAS u32x4*)(e + KO - 16) = ha;
        }
    }
    STAMP(8);
#pragma unroll
    for (int i = 0; i < 2; ++i) {
        const int it = i * 512 + tid, ch = it >> 8, q = it & 255;
        LAS char* f = (LAS char*)(lds + XY_OFF + (4 + ch) * KV) + 32 + q * 16;
        if (q == 0) kb[i].x = (kb[i].x & 0xffff0000u) | (pk2(lo2f(kb[i].x) + bf2f(q_sk[i]), 0.f) & 0xffffu);
        u32x4 dy = {__builtin_amdgcn_alignbit(kb[i].y, kb[i].x, 16), __builtin_amdgcn_alignbit(kb[i].z, kb[i].y, 16),
                    __builtin_amdgcn_alignbit(kb[i].w, kb[i].z, 16), __builtin_amdgcn_alignbit(kbn[i], kb[i].w, 16)};
        *(LAS u32x4*)f = kb[i]; *(LAS u32x4*)(f + KO) = dy;
        if (q == 0) {
            const u32x4 z = {0u, 0u, 0u, 0u};
            u32x4 hb = {0u, 0u, 0u, kb[i].x << 16};
            *(LAS u32x4*)(f - 32) = z; *(LAS u32x4*)(f - 16) = z; *(LAS u32x4*)(f + KO - 32) = z; *(LAS u32x4*)(f + KO - 16) = hb;
        }
    }
    if (tid < 240) { const u32x4 z = {0u, 0u, 0u, 0u}; *(LAS u32x4*)(lds + U_OFF + (tid / 30) * US + 4096 + (tid % 30) * 16) = z; }
    u32x2 gq;
    {
        {
            constexpr u32 LO = 0x05040100u, HI = 0x07060302u;
            LAS char* ub = (LAS char*)(lds + U_OFF) + 2 * (2044 - 4 * tid);
            *(LAS u32x2*)(ub + 0 * US) = u32x2{__builtin_amdgcn_perm(cu[2].x, cu[3].x, LO), __builtin_amdgcn_perm(cu[0].x, cu[1].x, LO)};
            *(LAS u32x2*)(ub + 1 * US) = u32x2{__builtin_amdgcn_perm(cu[2].x, cu[3].x, HI), __builtin_amdgcn_perm(cu[0].x, cu[1].x, HI)};
            *(LAS u32x2*)(ub + 2 * US) = u32x2{__builtin_amdgcn_perm(cu[2].y, cu[3].y, LO), __builtin_amdgcn_perm(cu[0].y, cu[1].y, LO)};
            *(LAS u32x2*)(ub + 3 * US) = u32x2{__builtin_amdgcn_perm(cu[2].y, cu[3].y, HI), __builtin_amdgcn_perm(cu[0].y, cu[1].y, HI)};
        }
        const GAS u64* fl = (const GAS u64*)(p.ws + WS_FLAG) + wave * 32 + (lane & 31);
#pragma unroll 1
        for (u32 spins = 0; spins < (1u << 20); ++spins) {
            const u64 f0 = __hip_atomic_load(fl, RLX);
            if (__all(f0 == tag)) break;
            __builtin_amdgcn_s_sleep(1);
        }
        STAMP(9);
        u32x4 d[2];
#pragma unroll
        for (int i = 0; i < 2; ++i) {
            d[i] = __builtin_amdgcn_raw_buffer_load_b128(wsr, (int)WS_PAY + cg * 16384 + wave * 2048 + (i * 64 + lane) * 16, 0, 16);
        }
        gq = __builtin_amdgcn_raw_buffer_load_b64(wsr, (int)WS_G + tid * 8, 0, 16);
#pragma unroll
        for (int i = 0; i < 2; ++i) {
            const int b = wave * 32 + i * 16 + (lane >> 2), q = lane & 3;
            *(LAS u32x4*)(lds + U_OFF + (4 + q) * US + 4080 - 16 * b) = d[i];
        }
    }
    __syncthreads();
    if (blk == 0 && tid == 0) __hip_atomic_store((GAS u64*)p.ws, tag, RLX);
    STAMP(10);
    f32x4 acc[8];
#pragma unroll
    for (int c = 0; c < 8; ++c) acc[c] = f32x4{0.f, 0.f, 0.f, 0.f};
    {
        const int j = lane & 15, kg = lane >> 4;
        const LAS char* ab = (const LAS char*)(lds + XY_OFF + wave * KV) + ((j & 1) ? 2 * (1 + j + 8 * kg) : KO + 2 * (j + 8 * kg));
        const LAS char* bb = (const LAS char*)(lds + U_OFF + wave * US) + 2 * (2032 - 16 * j + 8 * kg);
        bf16x8 A[2][8], B[2][8];
        auto ld = [&](int r, bf16x8* a_, bf16x8* b_) {
#pragma unroll
            for (int a = 0; a < 8; ++a) {
                const AQ LAS u32* s = (const AQ LAS u32*)(ab + 512 * a + 64 * r);
                u32x4 t = {s[0], s[1], s[2], s[3]};
                a_[a] = __builtin_bit_cast(bf16x8, t);
                b_[a] = *(const LAS bf16x8*)(bb - 512 * a + 64 * r);
            }
        };
        ld(0, A[0], B[0]);
#pragma unroll
        for (int r = 0; r < 8; ++r) {
            if (r < 7) ld(r + 1, A[(r + 1) & 1], B[(r + 1) & 1]);
#pragma unroll
            for (int b = 0; b < 8; ++b)
#pragma unroll
                for (int a = 0; a + b < 8; ++a)
                    acc[a + b] = __builtin_amdgcn_mfma_f32_16x16x32_bf16(A[r & 1][a], B[r & 1][b], acc[a + b], 0, 0, 0);
            if (r < 7) {
#pragma unroll
                for (int i = 0; i < 8; ++i) {
                    __builtin_amdgcn_sched_group_barrier(0x008, 2, 0);
                    __builtin_amdgcn_sched_group_barrier(0x100, NDS, 0);
                    __builtin_amdgcn_sched_group_barrier(0x008, 2, 0);
                }
                __builtin_amdgcn_sched_group_barrier(0x008, 4, 0);
            }
            __builtin_amdgcn_sched_barrier(0);
        }
    }
    STAMP(11);
    {
        const int n = lane & 15, kg = lane >> 4;
        const int sg = 4 * n + kg, ps = sg ^ ((sg >> 3) & 7);
        LAS char* rb = (LAS char*)(lds + XY_OFF + wave * KV) + ps * 16;
#pragma unroll
        for (int c = 0; c < 8; ++c) *(LAS f32x4*)(rb + c * 1024) = acc[c];
    }
    __syncthreads();
    STAMP(12);
    {
        const int ps = lane ^ ((lane >> 3) & 7);
        const LAS char* rb = (const LAS char*)(lds + XY_OFF) + wave * 1024 + ps * 16;
        const LAS char* ub = (const LAS char*)(lds + U_OFF) + 2 * (2044 - 4 * tid);
        const f32x2 g01 = {lo2f(gq.x), hi2f(gq.x)}, g23 = {lo2f(gq.y), hi2f(gq.y)};
        f32x2 o01[4], o23[4];
#pragma unroll
        for (int c = 0; c < 4; ++c) {
            const f32x4 a = *(const LAS f32x4*)(rb + c * KV), b = *(const LAS f32x4*)(rb + (4 + c) * KV);
            const u32x2 uu = *(const LAS u32x2*)(ub + c * US);
            const f32x2 u01 = {hi2f(uu.y), lo2f(uu.y)}, u23 = {hi2f(uu.x), lo2f(uu.x)};
            const f32x2 b01 = u01 * f32x2{b.x, b.y}, b23 = u23 * f32x2{b.z, b.w};
            o01[c] = g01 * (f32x2{a.x, a.y} - b01) + b01;
            o23[c] = g23 * (f32x2{a.z, a.w} - b23) + b23;
        }
        GAS char* ob = (GAS char*)(p.out + (size_t)(4 * tid) * DCH + 4 * cg);
        *(GAS u32x2*)(ob) = u32x2{pk2(o01[0].x, o01[1].x), pk2(o01[2].x, o01[3].x)};
        *(GAS u32x2*)(ob + 2 * DCH) = u32x2{pk2(o01[0].y, o01[1].y), pk2(o01[2].y, o01[3].y)};
        *(GAS u32x2*)(ob + 4 * DCH) = u32x2{pk2(o23[0].x, o23[1].x), pk2(o23[2].x, o23[3].x)};
        *(GAS u32x2*)(ob + 6 * DCH) = u32x2{pk2(o23[0].y, o23[1].y), pk2(o23[2].y, o23[3].y)};
    }
    STAMP(13);
}

extern "C" void kernel_launch(void* const* d_in, const int* in_sizes, int n_in, void* d_out, int out_size, void* d_ws,
                              size_t ws_size, hipStream_t stream) {
    Args a;
    a.u = (const u16*)d_in[0]; a.lar = (const u16*)d_in[1]; a.aim = (const u16*)d_in[2]; a.cr = (const u16*)d_in[3];
    a.ci = (const u16*)d_in[4]; a.ldt = (const u16*)d_in[5]; a.dsk = (const u16*)d_in[6]; a.k = (const u16*)d_in[8];
    a.v = (const u16*)d_in[9]; a.shk = (const u16*)d_in[10]; a.ssk = (const u16*)d_in[11]; a.ssd = (const u16*)d_in[12];
    a.gw = (const u16*)d_in[13]; a.out = (u16*)d_out; a.ws = (char*)d_ws;
    (void)hipFuncSetAttribute((const void*)mix_kernel<0>, hipFuncAttributeMaxDynamicSharedMemorySize, LDS_BYTES);
    mix_kernel<0><<<256, 512, LDS_BYTES, stream>>>(a);
}
```

```cpp
#define PFIRST
#define CODE_LINES 104
#include <hip/hip_runtime.h>
#include <stdint.h>

typedef unsigned short u16;
typedef unsigned int u32;
typedef unsigned long long u64;
typedef float f32x4 __attribute__((ext_vector_type(4)));
typedef float f32x2 __attribute__((ext_vector_type(2)));
typedef u32 u32x4 __attribute__((ext_vector_type(4)));
typedef u32 u32x2 __attribute__((ext_vector_type(2)));
typedef __bf16 bf16x8 __attribute__((ext_vector_type(8)));
typedef __bf16 bf16x2 __attribute__((ext_vector_type(2)));
#define GAS __attribute__((address_space(1)))
#define LAS __attribute__((address_space(3)))

constexpr int SEQ = 2048, DCH = 1024;
constexpr int US = 4624;
constexpr int U_OFF = 0;
constexpr int XY_OFF = 8 * US;
constexpr int XROW = 544;
constexpr int XCH = 53 * XROW;
constexpr int P_OFF = XY_OFF + 4 * XCH;
constexpr int PS = 644;
constexpr int R_OFF = P_OFF + 4 * PS * 4;
constexpr int LDS_BYTES = R_OFF + 128;
constexpr int KO = 4160;
constexpr int KV = 8320;
constexpr int TS = 136;
constexpr size_t WS_G = 4096, WS_FLAG = 65536, WS_PAY = 131072;

struct Args {
    const u16 *u, *lar, *aim, *cr, *ci, *ldt, *dsk, *k, *v, *shk, *ssk, *ssd, *gw;
    u16* out;
    char* ws;
};

__device__ __forceinline__ float bf2f(u32 h) { return __uint_as_float(h << 16); }
__device__ __forceinline__ float lo2f(u32 d) { return __uint_as_float(d << 16); }
__device__ __forceinline__ float hi2f(u32 d) { return __uint_as_float(d & 0xffff0000u); }
__device__ __forceinline__ u32 pk2(float a, float b) {
    bf16x2 r = {(__bf16)a, (__bf16)b};
    return __builtin_bit_cast(u32, r);
}
__device__ __forceinline__ float rbf(float a) { return lo2f(pk2(a, 0.f)); }
template <int C, int RM>
__device__ __forceinline__ float dpp_add(float v) {
    return v + __int_as_float(__builtin_amdgcn_update_dpp(0, __float_as_int(v), C, RM, 0xf, true));
}
__device__ __forceinline__ float wave_sum63(float v) {
    v = dpp_add<0x111, 0xf>(v); v = dpp_add<0x112, 0xf>(v); v = dpp_add<0x114, 0xf>(v); v = dpp_add<0x118, 0xf>(v);
    v = dpp_add<0x142, 0xa>(v); v = dpp_add<0x143, 0xc>(v);
    return v;
}
__device__ __forceinline__ f32x2 cmul(f32x2 a, f32x2 b) {
    f32x2 r;
    asm("v_pk_mul_f32 %0, %1, %2 op_sel:[0,0] op_sel_hi:[0,1]" : "=v"(r) : "v"(a), "v"(b));
    asm("v_pk_fma_f32 %0, %1, %2, %0 op_sel:[1,1,0] op_sel_hi:[1,0,1] neg_lo:[0,1,0]" : "+v"(r) : "v"(a), "v"(b));
    return r;
}
__device__ __forceinline__ f32x2 cexp(float ar, float ai, float p) {
    float m = __expf(ar * p);
    float rev = (ai * p) * 0.15915494309189535f;
    rev = __builtin_amdgcn_fractf(rev);
    f32x2 r; r.x = m * __builtin_amdgcn_cosf(rev); r.y = m * __builtin_amdgcn_sinf(rev); return r;
}
#define RLX __ATOMIC_RELAXED, __HIP_MEMORY_SCOPE_AGENT
constexpr size_t WS_PROF = 32u << 20;
#define STAMP(i)

#define AQ
#define NDS 3
template <int MODE>
__global__ void __launch_bounds__(512, 2) mix_kernel(Args p) {
    extern __shared__ __attribute__((aligned(16))) char lds[];
    const int tid = threadIdx.x, lane = tid & 63, wave = __builtin_amdgcn_readfirstlane(tid >> 6);
    const int blk = blockIdx.x;
    const int cg = (blk & 7) * 32 + (blk >> 3);
    const __amdgpu_buffer_rsrc_t wsr = __builtin_amdgcn_make_buffer_rsrc((void*)p.ws, 0, 0x7fffffff, 0x00020000);
    STAMP(0);

    const int gch = tid >> 7, gn = tid & 127;
    const int ph = tid >> 8, pc = tid & 255;
    u32x2 pu[4], pv[4], pk[7], pg; u32x4 pw0, pw1;
    u32 q_lar, q_aim, q_cr, q_ci, q_ldt;
    { const int o = (4 * cg + gch) * 128 + gn;
      q_lar = p.lar[o]; q_aim = p.aim[o]; q_cr = p.cr[o]; q_ci = p.ci[o]; q_ldt = p.ldt[4 * cg + gch]; }
    __builtin_amdgcn_sched_barrier(0);
    if (MODE != 2) {
        const int t0 = 8 * blk + 4 * ph;
        const GAS u32x2* U = (const GAS u32x2*)p.u; const GAS u32x2* V = (const GAS u32x2*)p.v; const GAS u32x2* K = (const GAS u32x2*)p.k;
#pragma unroll
        for (int i = 0; i < 7; ++i) {
            const int t = t0 - 3 + i;
            pk[i] = K[(t < 0 ? 0 : t) * 256 + pc];
        }
#pragma unroll
        for (int i = 0; i < 4; ++i) { pv[i] = V[(t0 + i) * 256 + pc]; pu[i] = U[(t0 + i) * 256 + pc]; }
        pw0 = ((const GAS u32x4*)p.shk)[2 * pc]; pw1 = ((const GAS u32x4*)p.shk)[2 * pc + 1];
        pg = ((const GAS u32x2*)p.gw)[pc];
    }
    __builtin_amdgcn_sched_barrier(0);
    STAMP(1);
    u32x4 kb[2]; u32 kbn[2]; u32 q_sk[2];
    const u32 q_dsk0 = p.dsk[4 * cg + (wave >> 1)];
    const u64 tag = __hip_atomic_load((GAS u64*)p.ws, RLX) + 1;
    u32 pf = 0;
    {
        u64 pc_; asm volatile("s_getpc_b64 %0" : "=s"(pc_));
        if (tid < CODE_LINES) pf = *(const GAS u32*)((pc_ & ~255ull) + tid * 128);
    }
    __builtin_amdgcn_sched_barrier(0);
    {
        const float dt = rbf(expf(bf2f(q_ldt)));
        const float ar = -rbf(expf(bf2f(q_lar))) * dt, ai = bf2f(q_aim) * dt;
        const f32x2 X0 = {2.f * (bf2f(q_cr) * dt), 2.f * (bf2f(q_ci) * dt)};
        LAS u32* xb = (LAS u32*)(lds + XY_OFF + gch * XCH) + gn;
        constexpr int RW = XROW / 4;
        const f32x2 z = cexp(ar, ai, 1.f), w = cexp(ar, ai, 8.f), w32 = cexp(ar, ai, 256.f), e1024 = cexp(ar, ai, 1024.f);
        f32x2 X[8]; X[0] = X0;
#pragma unroll
        for (int r = 1; r < 8; ++r) X[r] = cmul(X[r - 1], z);
#pragma unroll
        for (int r = 0; r < 8; ++r) xb[r * RW] = pk2(X[r].x, X[r].y);
#pragma unroll
        for (int r = 0; r < 4; ++r) { f32x2 t = cmul(X[2 * r], w32); xb[(8 + r) * RW] = pk2(t.x, t.y); }
        const f32x2 e512 = cmul(w32, w32), e768 = cmul(e512, w32);
        { f32x2 t = cmul(X[0], e512); xb[12 * RW] = pk2(t.x, t.y); t = cmul(X[4], e512); xb[13 * RW] = pk2(t.x, t.y);
          t = cmul(X[0], e768); xb[14 * RW] = pk2(t.x, t.y); t = cmul(X[4], e768); xb[15 * RW] = pk2(t.x, t.y); }
        { const f32x2 x16 = cmul(X[0], e1024); xb[16 * RW] = pk2(x16.x, x16.y);
          f32x2 t = cmul(x16, w32); xb[17 * RW] = pk2(t.x, t.y);
          t = cmul(x16, e512); xb[18 * RW] = pk2(t.x, t.y);
          t = cmul(x16, e768); xb[19 * RW] = pk2(t.x, t.y);
          t = cmul(x16, e1024); xb[20 * RW] = pk2(t.x, t.y); }
        const f32x2 wc = {w.x, -w.y};
        const f32x2 w2 = cmul(wc, wc);
        f32x2 y0 = {1.f, 0.f}, y1 = wc;
#pragma unroll
        for (int j = 0; j < 32; j += 2) {
            xb[(21 + j) * RW] = pk2(y0.x, y0.y); xb[(22 + j) * RW] = pk2(y1.x, y1.y);
            if (j < 30) { y0 = cmul(y0, w2); y1 = cmul(y1, w2); }
        }
    }
    __builtin_amdgcn_sched_barrier(0);
    STAMP(5);
    if (MODE != 2) {
        if (blk == 0 && ph == 0) { pk[0] = u32x2{0u, 0u}; pk[1] = pk[0]; pk[2] = pk[0]; }
        f32x2 wa[4], wb[4];
        wa[0] = f32x2{lo2f(pw0.x), lo2f(pw0.z)}; wa[1] = f32x2{hi2f(pw0.x), hi2f(pw0.z)};
        wa[2] = f32x2{lo2f(pw0.y), lo2f(pw0.w)}; wa[3] = f32x2{hi2f(pw0.y), hi2f(pw0.w)};
        wb[0] = f32x2{lo2f(pw1.x), lo2f(pw1.z)}; wb[1] = f32x2{hi2f(pw1.x), hi2f(pw1.z)};
        wb[2] = f32x2{lo2f(pw1.y), lo2f(pw1.w)}; wb[3] = f32x2{hi2f(pw1.y), hi2f(pw1.w)};
        f32x2 ka[7], kc[7], kva[4], kvc[4]; float gs[4];
#pragma unroll
        for (int i = 0; i < 7; ++i) { ka[i] = f32x2{lo2f(pk[i].x), hi2f(pk[i].x)}; kc[i] = f32x2{lo2f(pk[i].y), hi2f(pk[i].y)}; }
        const f32x2 ga = {lo2f(pg.x), hi2f(pg.x)}, gc = {lo2f(pg.y), hi2f(pg.y)};
#pragma unroll
        for (int i = 0; i < 4; ++i) {
            const f32x2 va = {lo2f(pv[i].x), hi2f(pv[i].x)}, vc = {lo2f(pv[i].y), hi2f(pv[i].y)};
            kva[i] = (ka[i] * wa[3] + ka[i + 1] * wa[2] + ka[i + 2] * wa[1] + ka[i + 3] * wa[0]) * va;
            kvc[i] = (kc[i] * wb[3] + kc[i + 1] * wb[2] + kc[i + 2] * wb[1] + kc[i + 3] * wb[0]) * vc;
            const f32x2 ua = {lo2f(pu[i].x), hi2f(pu[i].x)}, uc = {lo2f(pu[i].y), hi2f(pu[i].y)};
            const f32x2 m = ua * ga + uc * gc;
            gs[i] = m.x + m.y;
        }
        LAS char* tb = (LAS char*)(lds + U_OFF) + pc * TS + (1 - ph) * 8;
        constexpr u32 LO = 0x05040100u, HI = 0x07060302u;
        *(LAS u32x2*)(tb + 0) = u32x2{__builtin_amdgcn_perm(pu[2].x, pu[3].x, LO), __builtin_amdgcn_perm(pu[0].x, pu[1].x, LO)};
        *(LAS u32x2*)(tb + 16) = u32x2{__builtin_amdgcn_perm(pu[2].x, pu[3].x, HI), __builtin_amdgcn_perm(pu[0].x, pu[1].x, HI)};
        *(LAS u32x2*)(tb + 32) = u32x2{__builtin_amdgcn_perm(pu[2].y, pu[3].y, LO), __builtin_amdgcn_perm(pu[0].y, pu[1].y, LO)};
        *(LAS u32x2*)(tb + 48) = u32x2{__builtin_amdgcn_perm(pu[2].y, pu[3].y, HI), __builtin_amdgcn_perm(pu[0].y, pu[1].y, HI)};
        *(LAS u32x2*)(tb + 64) = u32x2{pk2(kva[3].x, kva[2].x), pk2(kva[1].x, kva[0].x)};
        *(LAS u32x2*)(tb + 80) = u32x2{pk2(kva[3].y, kva[2].y), pk2(kva[1].y, kva[0].y)};
        *(LAS u32x2*)(tb + 96) = u32x2{pk2(kvc[3].x, kvc[2].x), pk2(kvc[1].x, kvc[0].x)};
        *(LAS u32x2*)(tb + 112) = u32x2{pk2(kvc[3].y, kvc[2].y), pk2(kvc[1].y, kvc[0].y)};
#pragma unroll
        for (int i = 0; i < 4; ++i) gs[i] = dpp_add<0x111, 0xf>(gs[i]);
#pragma unroll
        for (int i = 0; i < 4; ++i) gs[i] = dpp_add<0x112, 0xf>(gs[i]);
#pragma unroll
        for (int i = 0; i < 4; ++i) gs[i] = dpp_add<0x114, 0xf>(gs[i]);
#pragma unroll
        for (int i = 0; i < 4; ++i) gs[i] = dpp_add<0x118, 0xf>(gs[i]);
#pragma unroll
        for (int i = 0; i < 4; ++i) gs[i] = dpp_add<0x142, 0xa>(gs[i]);
#pragma unroll
        for (int i = 0; i < 4; ++i) gs[i] = dpp_add<0x143, 0xc>(gs[i]);
        if (lane == 63) { f32x4 s = {gs[0], gs[1], gs[2], gs[3]}; *(LAS f32x4*)(lds + R_OFF + wave * 16) = s; }
        STAMP(2);
        __syncthreads();
        STAMP(3);
#pragma unroll
        for (int i = 0; i < 4; ++i) {
            const int P = i * 512 + tid, c = P >> 3, q = P & 7;
            const LAS u32x2* s = (const LAS u32x2*)(lds + U_OFF + c * TS + q * 16);
            const u32x2 a = s[0], b = s[1];
            u32x4 d = {a.x, a.y, b.x, b.y};
            __builtin_amdgcn_raw_buffer_store_b128(d, wsr, (int)WS_PAY + c * 32768 + blk * 128 + q * 16, 0, 16);
        }
        if (wave == 7) {
            const int r = lane & 7, hh = r >> 2, ii = r & 3;
            const LAS float* s = (const LAS float*)(lds + R_OFF) + hh * 16 + ii;
            const float sum = rbf((s[0] + s[4]) + (s[8] + s[12]));
            const float g = __builtin_amdgcn_rcpf(1.f + __expf(-sum));
            const u32 gb = pk2(g, 0.f) & 0xffffu;
            u32x4 d;
            d.x = __builtin_amdgcn_readlane(gb, 0) | (__builtin_amdgcn_readlane(gb, 1) << 16);
            d.y = __builtin_amdgcn_readlane(gb, 2) | (__builtin_amdgcn_readlane(gb, 3) << 16);
            d.z = __builtin_amdgcn_readlane(gb, 4) | (__builtin_amdgcn_readlane(gb, 5) << 16);
            d.w = __builtin_amdgcn_readlane(gb, 6) | (__builtin_amdgcn_readlane(gb, 7) << 16);
            if (lane == 0) __builtin_amdgcn_raw_buffer_store_b128(d, wsr, (int)WS_G + blk * 16, 0, 16);
        }
    }
    STAMP(4);
    if (MODE == 1) return;
    {
        const int ch = wave >> 1, ct = wave & 1;
        const LAS char* xb = (const LAS char*)(lds + XY_OFF + ch * XCH);
        const LAS char* a0 = xb + (lane & 15) * XROW + (lane >> 4) * 16;
        const LAS char* b0 = xb + (21 + ct * 16 + (lane & 15)) * XROW + (lane >> 4) * 16;
        f32x4 d0 = {0.f, 0.f, 0.f, 0.f}, d1 = d0;
#pragma unroll
        for (int ks = 0; ks < 8; ++ks) {
            const bf16x8 fa0 = *(const LAS bf16x8*)(a0 + ks * 64), fa1 = *(const LAS bf16x8*)(a0 + 16 * XROW + ks * 64);
            const bf16x8 fb = *(const LAS bf16x8*)(b0 + ks * 64);
            d0 = __builtin_amdgcn_mfma_f32_16x16x32_bf16(fa0, fb, d0, 0, 0, 0);
            d1 = __builtin_amdgcn_mfma_f32_16x16x32_bf16(fa1, fb, d1, 0, 0, 0);
        }
        LAS float* P = (LAS float*)(lds + P_OFF) + ch * PS;
        const int j = ct * 16 + (lane & 15), rg = lane >> 4;
        if (lane == 0 && ct == 0) d0[0] += bf2f(q_dsk0);
        if (rg < 2) {
#pragma unroll
            for (int i = 0; i < 4; ++i) P[4 * rg + i + 8 * j] = d0[i];
        } else if (rg == 2) {
#pragma unroll
            for (int i = 0; i < 4; ++i) P[256 + i + 4 * j] = d0[i];
        } else {
            P[384 + 2 * j] = d0[0]; P[385 + 2 * j] = d0[1]; P[448 + 2 * j] = d0[2]; P[449 + 2 * j] = d0[3];
        }
        if (rg == 0) {
#pragma unroll
            for (int i = 0; i < 4; ++i) P[512 + 32 * i + j] = d1[i];
        } else if (rg == 1 && j == 0) P[640] = d1[0];
    }
    STAMP(7);
    asm volatile("" :: "v"(pf));
    if (MODE == 0) asm volatile("s_waitcnt vmcnt(0)" ::: "memory");
    __syncthreads();
    if (tid < 32) __hip_atomic_store((GAS u64*)(p.ws + WS_FLAG) + tid * 256 + blk, tag, RLX);
    __builtin_amdgcn_sched_barrier(0);
#pragma unroll
    for (int i = 0; i < 2; ++i) {
        const int it = i * 512 + tid, ch = it >> 8, q = it & 255;
        const GAS u32* s = (const GAS u32*)(p.ssk + (4 * cg + ch) * SEQ) + 4 * q;
        kb[i] = *(const GAS u32x4*)s;
        kbn[i] = s[q < 255 ? 4 : 0];
        q_sk[i] = p.ssd[4 * cg + ch];
    }
    __builtin_amdgcn_sched_barrier(0);
    STAMP(6);
#pragma unroll
    for (int i = 0; i < 2; ++i) {
        const int it = i * 512 + tid, ch = it >> 8, q = it & 255;
        const LAS float* P = (const LAS float*)(lds + P_OFF) + ch * PS;
        float k[9];
        if (q < 32) {
#pragma unroll
            for (int e = 0; e < 9; ++e) k[e] = P[8 * q + e];
        } else if (q < 64) {
            const LAS float* s = P + 256 + 4 * (q - 32);
            const float v0 = s[0], v1 = s[1], v2 = s[2], v3 = s[3], v4 = s[4];
            k[0] = v0; k[1] = v0; k[2] = v1; k[3] = v2; k[4] = v2; k[5] = v2; k[6] = v3; k[7] = v4; k[8] = v4;
        } else if (q < 128) {
            const LAS float* s = P + 384 + 2 * (q - 64);
            const float v0 = s[0], v1 = s[1], v2 = s[2];
            k[0] = v0; k[1] = v0; k[2] = v0; k[3] = v1; k[4] = v1; k[5] = v1; k[6] = v2; k[7] = v2; k[8] = v2;
        } else {
            const LAS float* s = P + 512 + (q - 128);
            const float v0 = s[0], v1 = s[1];
            k[0] = v0; k[1] = v0; k[2] = v0; k[3] = v0; k[4] = (q & 1) ? v1 : v0; k[5] = v1; k[6] = v1; k[7] = v1; k[8] = v1;
        }
        LAS char* e = (LAS char*)(lds + XY_OFF + ch * KV) + 32 + q * 16;
        u32x4 de = {pk2(k[0], k[1]), pk2(k[2], k[3]), pk2(k[4], k[5]), pk2(k[6], k[7])};
        u32x4 dz = {pk2(k[1], k[2]), pk2(k[3], k[4]), pk2(k[5], k[6]), pk2(k[7], k[8])};
        *(LAS u32x4*)e = de; *(LAS u32x4*)(e + KO) = dz;
        if (q == 0) {
            const u32x4 z = {0u, 0u, 0u, 0u};
            u32x4 ha = {0u, 0u, 0u, pk2(0.f, k[0])};
            *(LAS u32x4*)(e - 32) = z; *(LAS u32x4*)(e - 16) = z; *(LAS u32x4*)(e + KO - 32) = z; *(LAS u32x4*)(e + KO - 16) = ha;
        }
    }
    STAMP(8);
#pragma unroll
    for (int i = 0; i < 2; ++i) {
        const int it = i * 512 + tid, ch = it >> 8, q = it & 255;
        LAS char* f = (LAS char*)(lds + XY_OFF + (4 + ch) * KV) + 32 + q * 16;
        if (q == 0) kb[i].x = (kb[i].x & 0xffff0000u) | (pk2(lo2f(kb[i].x) + bf2f(q_sk[i]), 0.f) & 0xffffu);
        u32x4 dy = {__builtin_amdgcn_alignbit(kb[i].y, kb[i].x, 16), __builtin_amdgcn_alignbit(kb[i].z, kb[i].y, 16),
                    __builtin_amdgcn_alignbit(kb[i].w, kb[i].z, 16), __builtin_amdgcn_alignbit(kbn[i], kb[i].w, 16)};
        *(LAS u32x4*)f = kb[i]; *(LAS u32x4*)(f + KO) = dy;
        if (q == 0) {
            const u32x4 z = {0u, 0u, 0u, 0u};
            u32x4 hb = {0u, 0u, 0u, kb[i].x << 16};
            *(LAS u32x4*)(f - 32) = z; *(LAS u32x4*)(f - 16) = z; *(LAS u32x4*)(f + KO - 32) = z; *(LAS u32x4*)(f + KO - 16) = hb;
        }
    }
    if (tid < 240) { const u32x4 z = {0u, 0u, 0u, 0u}; *(LAS u32x4*)(lds + U_OFF + (tid / 30) * US + 4096 + (tid % 30) * 16) = z; }
    u32x2 gq;
    {
        const GAS u64* fl = (const GAS u64*)(p.ws + WS_FLAG) + (blk & 31) * 256 + wave * 32 + (lane & 31);
#pragma unroll 1
        for (u32 spins = 0; spins < (1u << 20); ++spins) {
            const u64 f0 = __hip_atomic_load(fl, RLX);
            if (__all(f0 == tag)) break;
            __builtin_amdgcn_s_sleep(1);
        }
        STAMP(9);
        u32x4 d[4];
#pragma unroll
        for (int i = 0; i < 4; ++i) d[i] = __builtin_amdgcn_raw_buffer_load_b128(wsr, (int)WS_PAY + cg * 32768 + wave * 4096 + (i * 64 + lane) * 16, 0, 16);
        gq = __builtin_amdgcn_raw_buffer_load_b64(wsr, (int)WS_G + tid * 8, 0, 16);
#pragma unroll
        for (int i = 0; i < 4; ++i) {
            const int b = wave * 32 + i * 8 + (lane >> 3), q = lane & 7;
            *(LAS u32x4*)(lds + U_OFF + q * US + 4080 - 16 * b) = d[i];
        }
    }
    __syncthreads();
    if (blk == 0 && tid == 0) __hip_atomic_store((GAS u64*)p.ws, tag, RLX);
    STAMP(10);
    f32x4 acc[8];
#pragma unroll
    for (int c = 0; c < 8; ++c) acc[c] = f32x4{0.f, 0.f, 0.f, 0.f};
    {
        const int j = lane & 15, kg = lane >> 4;
        const LAS char* ab = (const LAS char*)(lds + XY_OFF + wave * KV) + ((j & 1) ? 2 * (1 + j + 8 * kg) : KO + 2 * (j + 8 * kg));
        const LAS char* bb = (const LAS char*)(lds + U_OFF + wave * US) + 2 * (2032 - 16 * j + 8 * kg);
        bf16x8 A[2][8], B[2][8];
        auto ld = [&](int r, bf16x8* a_, bf16x8* b_) {
#pragma unroll
            for (int a = 0; a < 8; ++a) {
                const AQ LAS u32* s = (const AQ LAS u32*)(ab + 512 * a + 64 * r);
                u32x4 t = {s[0], s[1], s[2], s[3]};
                a_[a] = __builtin_bit_cast(bf16x8, t);
                b_[a] = *(const LAS bf16x8*)(bb - 512 * a + 64 * r);
            }
        };
        ld(0, A[0], B[0]);
#pragma unroll
        for (int r = 0; r < 8; ++r) {
            if (r < 7) ld(r + 1, A[(r + 1) & 1], B[(r + 1) & 1]);
#pragma unroll
            for (int b = 0; b < 8; ++b)
#pragma unroll
                for (int a = 0; a + b < 8; ++a)
                    acc[a + b] = __builtin_amdgcn_mfma_f32_16x16x32_bf16(A[r & 1][a], B[r & 1][b], acc[a + b], 0, 0, 0);
            if (r < 7) {
#pragma unroll
                for (int i = 0; i < 8; ++i) {
                    __builtin_amdgcn_sched_group_barrier(0x008, 2, 0);
                    __builtin_amdgcn_sched_group_barrier(0x100, NDS, 0);
                    __builtin_amdgcn_sched_group_barrier(0x008, 2, 0);
                }
                __builtin_amdgcn_sched_group_barrier(0x008, 4, 0);
            }
            __builtin_amdgcn_sched_barrier(0);
        }
    }
    STAMP(11);
    {
        const int n = lane & 15, kg = lane >> 4;
        const int sg = 4 * n + kg, ps = sg ^ ((sg >> 3) & 7);
        LAS char* rb = (LAS char*)(lds + XY_OFF + wave * KV) + ps * 16;
#pragma unroll
        for (int c = 0; c < 8; ++c) *(LAS f32x4*)(rb + c * 1024) = acc[c];
    }
    __syncthreads();
    STAMP(12);
    {
        const int ps = lane ^ ((lane >> 3) & 7);
        const LAS char* rb = (const LAS char*)(lds + XY_OFF) + wave * 1024 + ps * 16;
        const LAS char* ub = (const LAS char*)(lds + U_OFF) + 2 * (2044 - 4 * tid);
        const f32x2 g01 = {lo2f(gq.x), hi2f(gq.x)}, g23 = {lo2f(gq.y), hi2f(gq.y)};
        f32x2 o01[4], o23[4];
#pragma unroll
        for (int c = 0; c < 4; ++c) {
            const f32x4 a = *(const LAS f32x4*)(rb + c * KV), b = *(const LAS f32x4*)(rb + (4 + c) * KV);
            const u32x2 uu = *(const LAS u32x2*)(ub + c * US);
            const f32x2 u01 = {hi2f(uu.y), lo2f(uu.y)}, u23 = {hi2f(uu.x), lo2f(uu.x)};
            const f32x2 b01 = u01 * f32x2{b.x, b.y}, b23 = u23 * f32x2{b.z, b.w};
            o01[c] = g01 * (f32x2{a.x, a.y} - b01) + b01;
            o23[c] = g23 * (f32x2{a.z, a.w} - b23) + b23;
        }
        GAS char* ob = (GAS char*)(p.out + (size_t)(4 * tid) * DCH + 4 * cg);
        *(GAS u32x2*)(ob) = u32x2{pk2(o01[0].x, o01[1].x), pk2(o01[2].x, o01[3].x)};
        *(GAS u32x2*)(ob + 2 * DCH) = u32x2{pk2(o01[0].y, o01[1].y), pk2(o01[2].y, o01[3].y)};
        *(GAS u32x2*)(ob + 4 * DCH) = u32x2{pk2(o23[0].x, o23[1].x), pk2(o23[2].x, o23[3].x)};
        *(GAS u32x2*)(ob + 6 * DCH) = u32x2{pk2(o23[0].y, o23[1].y), pk2(o23[2].y, o23[3].y)};
    }
    STAMP(13);
}

extern "C" void kernel_launch(void* const* d_in, const int* in_sizes, int n_in, void* d_out, int out_size, void* d_ws,
                              size_t ws_size, hipStream_t stream) {
    Args a;
    a.u = (const u16*)d_in[0]; a.lar = (const u16*)d_in[1]; a.aim = (const u16*)d_in[2]; a.cr = (const u16*)d_in[3];
    a.ci = (const u16*)d_in[4]; a.ldt = (const u16*)d_in[5]; a.dsk = (const u16*)d_in[6]; a.k = (const u16*)d_in[8];
    a.v = (const u16*)d_in[9]; a.shk = (const u16*)d_in[10]; a.ssk = (const u16*)d_in[11]; a.ssd = (const u16*)d_in[12];
    a.gw = (const u16*)d_in[13]; a.out = (u16*)d_out; a.ws = (char*)d_ws;
    (void)hipFuncSetAttribute((const void*)mix_kernel<0>, hipFuncAttributeMaxDynamicSharedMemorySize, LDS_BYTES);
    mix_kernel<0><<<256, 512, LDS_BYTES, stream>>>(a);
}
```

```cpp
#define PFIRST
#define CODE_LINES 100
#include <hip/hip_runtime.h>
#include <stdint.h>

typedef unsigned short u16;
typedef unsigned int u32;
typedef unsigned long long u64;
typedef float f32x4 __attribute__((ext_vector_type(4)));
typedef float f32x2 __attribute__((ext_vector_type(2)));
typedef u32 u32x4 __attribute__((ext_vector_type(4)));
typedef u32 u32x2 __attribute__((ext_vector_type(2)));
typedef __bf16 bf16x8 __attribute__((ext_vector_type(8)));
typedef __bf16 bf16x2 __attribute__((ext_vector_type(2)));
#define GAS __attribute__((address_space(1)))
#define LAS __attribute__((address_space(3)))

constexpr int SEQ = 2048, DCH = 1024;
constexpr int US = 4624;
constexpr int U_OFF = 0;
constexpr int XY_OFF = 8 * US;
constexpr int XROW = 544;
constexpr int XCH = 53 * XROW;
constexpr int P_OFF = XY_OFF + 4 * XCH;
constexpr int PS = 644;
constexpr int R_OFF = P_OFF + 4 * PS * 4;
constexpr int LDS_BYTES = R_OFF + 256;
constexpr int KO = 4160;
constexpr int KV = 8320;
constexpr int TS = 136;
constexpr size_t WS_G = 4096, WS_FLAG = 65536, WS_PAY = 131072;

struct Args {
    const u16 *u, *lar, *aim, *cr, *ci, *ldt, *dsk, *k, *v, *shk, *ssk, *ssd, *gw;
    u16* out;
    char* ws;
};

__device__ __forceinline__ float bf2f(u32 h) { return __uint_as_float(h << 16); }
__device__ __forceinline__ float lo2f(u32 d) { return __uint_as_float(d << 16); }
__device__ __forceinline__ float hi2f(u32 d) { return __uint_as_float(d & 0xffff0000u); }
__device__ __forceinline__ u32 pk2(float a, float b) {
    bf16x2 r = {(__bf16)a, (__bf16)b};
    return __builtin_bit_cast(u32, r);
}
__device__ __forceinline__ float rbf(float a) { return lo2f(pk2(a, 0.f)); }
template <int C, int RM>
__device__ __forceinline__ float dpp_add(float v) {
    return v + __int_as_float(__builtin_amdgcn_update_dpp(0, __float_as_int(v), C, RM, 0xf, true));
}
__device__ __forceinline__ float wave_sum63(float v) {
    v = dpp_add<0x111, 0xf>(v); v = dpp_add<0x112, 0xf>(v); v = dpp_add<0x114, 0xf>(v); v = dpp_add<0x118, 0xf>(v);
    v = dpp_add<0x142, 0xa>(v); v = dpp_add<0x143, 0xc>(v);
    return v;
}
__device__ __forceinline__ f32x2 cmul(f32x2 a, f32x2 b) {
    f32x2 r;
    asm("v_pk_mul_f32 %0, %1, %2 op_sel:[0,0] op_sel_hi:[0,1]" : "=v"(r) : "v"(a), "v"(b));
    asm("v_pk_fma_f32 %0, %1, %2, %0 op_sel:[1,1,0] op_sel_hi:[1,0,1] neg_lo:[0,1,0]" : "+v"(r) : "v"(a), "v"(b));
    return r;
}
__device__ __forceinline__ f32x2 cexp(float ar, float ai, float p) {
    float m = __expf(ar * p);
    float rev = (ai * p) * 0.15915494309189535f;
    rev = __builtin_amdgcn_fractf(rev);
    f32x2 r; r.x = m * __builtin_amdgcn_cosf(rev); r.y = m * __builtin_amdgcn_sinf(rev); return r;
}
#define RLX __ATOMIC_RELAXED, __HIP_MEMORY_SCOPE_AGENT
constexpr size_t WS_PROF = 32u << 20;
#define STAMP(i)

#define AQ
#define NDS 3
template <int MODE>
__global__ void __launch_bounds__(512, 2) mix_kernel(Args p) {
    extern __shared__ __attribute__((aligned(16))) char lds[];
    const int tid = threadIdx.x, lane = tid & 63, wave = __builtin_amdgcn_readfirstlane(tid >> 6);
    const int blk = blockIdx.x;
    const int cg = (blk & 7) * 32 + (blk >> 3);
    const __amdgpu_buffer_rsrc_t wsr = __builtin_amdgcn_make_buffer_rsrc((void*)p.ws, 0, 0x7fffffff, 0x00020000);
    STAMP(0);

    const int gch = tid >> 7, gn = tid & 127;
    const int ph = lane >> 5, pc = wave * 32 + (lane & 31);
    u32x2 pu[4], pv[4], pk[7], pg; u32x4 pw0, pw1;
    u32 q_lar, q_aim, q_cr, q_ci, q_ldt;
    { const int o = (4 * cg + gch) * 128 + gn;
      q_lar = p.lar[o]; q_aim = p.aim[o]; q_cr = p.cr[o]; q_ci = p.ci[o]; q_ldt = p.ldt[4 * cg + gch]; }
    __builtin_amdgcn_sched_barrier(0);
    if (MODE != 2) {
        const int t0 = 8 * blk + 4 * ph;
        const GAS u32x2* U = (const GAS u32x2*)p.u; const GAS u32x2* V = (const GAS u32x2*)p.v; const GAS u32x2* K = (const GAS u32x2*)p.k;
#pragma unroll
        for (int i = 0; i < 7; ++i) {
            const int t = t0 - 3 + i;
            pk[i] = K[(t < 0 ? 0 : t) * 256 + pc];
        }
#pragma unroll
        for (int i = 0; i < 4; ++i) { pv[i] = V[(t0 + i) * 256 + pc]; pu[i] = U[(t0 + i) * 256 + pc]; }
        pw0 = ((const GAS u32x4*)p.shk)[2 * pc]; pw1 = ((const GAS u32x4*)p.shk)[2 * pc + 1];
        pg = ((const GAS u32x2*)p.gw)[pc];
    }
    __builtin_amdgcn_sched_barrier(0);
    STAMP(1);
    u32x4 kb[2]; u32 kbn[2]; u32 q_sk[2];
    const u32 q_dsk0 = p.dsk[4 * cg + (wave >> 1)];
    const u64 tag = __hip_atomic_load((GAS u64*)p.ws, RLX) + 1;
    u32 pf = 0;
    {
        u64 pc_; asm volatile("s_getpc_b64 %0" : "=s"(pc_));
        if (tid < CODE_LINES) pf = *(const GAS u32*)((pc_ & ~255ull) + tid * 128);
    }
    __builtin_amdgcn_sched_barrier(0);
    {
        const float dt = rbf(expf(bf2f(q_ldt)));
        const float ar = -rbf(expf(bf2f(q_lar))) * dt, ai = bf2f(q_aim) * dt;
        const f32x2 X0 = {2.f * (bf2f(q_cr) * dt), 2.f * (bf2f(q_ci) * dt)};
        LAS u32* xb = (LAS u32*)(lds + XY_OFF + gch * XCH) + gn;
        constexpr int RW = XROW / 4;
        const f32x2 z = cexp(ar, ai, 1.f), w = cexp(ar, ai, 8.f), w32 = cexp(ar, ai, 256.f), e1024 = cexp(ar, ai, 1024.f);
        f32x2 X[8]; X[0] = X0;
#pragma unroll
        for (int r = 1; r < 8; ++r) X[r] = cmul(X[r - 1], z);
#pragma unroll
        for (int r = 0; r < 8; ++r) xb[r * RW] = pk2(X[r].x, X[r].y);
#pragma unroll
        for (int r = 0; r < 4; ++r) { f32x2 t = cmul(X[2 * r], w32); xb[(8 + r) * RW] = pk2(t.x, t.y); }
        const f32x2 e512 = cmul(w32, w32), e768 = cmul(e512, w32);
        { f32x2 t = cmul(X[0], e512); xb[12 * RW] = pk2(t.x, t.y); t = cmul(X[4], e512); xb[13 * RW] = pk2(t.x, t.y);
          t = cmul(X[0], e768); xb[14 * RW] = pk2(t.x, t.y); t = cmul(X[4], e768); xb[15 * RW] = pk2(t.x, t.y); }
        { const f32x2 x16 = cmul(X[0], e1024); xb[16 * RW] = pk2(x16.x, x16.y);
          f32x2 t = cmul(x16, w32); xb[17 * RW] = pk2(t.x, t.y);
          t = cmul(x16, e512); xb[18 * RW] = pk2(t.x, t.y);
          t = cmul(x16, e768); xb[19 * RW] = pk2(t.x, t.y);
          t = cmul(x16, e1024); xb[20 * RW] = pk2(t.x, t.y); }
        const f32x2 wc = {w.x, -w.y};
        const f32x2 w2 = cmul(wc, wc);
        f32x2 y0 = {1.f, 0.f}, y1 = wc;
#pragma unroll
        for (int j = 0; j < 32; j += 2) {
            xb[(21 + j) * RW] = pk2(y0.x, y0.y); xb[(22 + j) * RW] = pk2(y1.x, y1.y);
            if (j < 30) { y0 = cmul(y0, w2); y1 = cmul(y1, w2); }
        }
    }
    __builtin_amdgcn_sched_barrier(0);
#pragma unroll
    for (int i = 0; i < 2; ++i) {
        const int it = i * 512 + tid, ch = it >> 8, q = it & 255;
        const GAS u32* s = (const GAS u32*)(p.ssk + (4 * cg + ch) * SEQ) + 4 * q;
        kb[i] = *(const GAS u32x4*)s;
        kbn[i] = s[q < 255 ? 4 : 0];
        q_sk[i] = p.ssd[4 * cg + ch];
    }
    __builtin_amdgcn_sched_barrier(0);
    STAMP(5);
    if (MODE != 2) {
        if (blk == 0 && ph == 0) { pk[0] = u32x2{0u, 0u}; pk[1] = pk[0]; pk[2] = pk[0]; }
        f32x2 wa[4], wb[4];
        wa[0] = f32x2{lo2f(pw0.x), lo2f(pw0.z)}; wa[1] = f32x2{hi2f(pw0.x), hi2f(pw0.z)};
        wa[2] = f32x2{lo2f(pw0.y), lo2f(pw0.w)}; wa[3] = f32x2{hi2f(pw0.y), hi2f(pw0.w)};
        wb[0] = f32x2{lo2f(pw1.x), lo2f(pw1.z)}; wb[1] = f32x2{hi2f(pw1.x), hi2f(pw1.z)};
        wb[2] = f32x2{lo2f(pw1.y), lo2f(pw1.w)}; wb[3] = f32x2{hi2f(pw1.y), hi2f(pw1.w)};
        f32x2 ka[7], kc[7], kva[4], kvc[4]; float gs[4];
#pragma unroll
        for (int i = 0; i < 7; ++i) { ka[i] = f32x2{lo2f(pk[i].x), hi2f(pk[i].x)}; kc[i] = f32x2{lo2f(pk[i].y), hi2f(pk[i].y)}; }
        const f32x2 ga = {lo2f(pg.x), hi2f(pg.x)}, gc = {lo2f(pg.y), hi2f(pg.y)};
#pragma unroll
        for (int i = 0; i < 4; ++i) {
            const f32x2 va = {lo2f(pv[i].x), hi2f(pv[i].x)}, vc = {lo2f(pv[i].y), hi2f(pv[i].y)};
            kva[i] = (ka[i] * wa[3] + ka[i + 1] * wa[2] + ka[i + 2] * wa[1] + ka[i + 3] * wa[0]) * va;
            kvc[i] = (kc[i] * wb[3] + kc[i + 1] * wb[2] + kc[i + 2] * wb[1] + kc[i + 3] * wb[0]) * vc;
            const f32x2 ua = {lo2f(pu[i].x), hi2f(pu[i].x)}, uc = {lo2f(pu[i].y), hi2f(pu[i].y)};
            const f32x2 m = ua * ga + uc * gc;
            gs[i] = m.x + m.y;
        }
        LAS char* tb = (LAS char*)(lds + U_OFF) + pc * TS + (1 - ph) * 8;
        constexpr u32 LO = 0x05040100u, HI = 0x07060302u;
        *(LAS u32x2*)(tb + 0) = u32x2{__builtin_amdgcn_perm(pu[2].x, pu[3].x, LO), __builtin_amdgcn_perm(pu[0].x, pu[1].x, LO)};
        *(LAS u32x2*)(tb + 16) = u32x2{__builtin_amdgcn_perm(pu[2].x, pu[3].x, HI), __builtin_amdgcn_perm(pu[0].x, pu[1].x, HI)};
        *(LAS u32x2*)(tb + 32) = u32x2{__builtin_amdgcn_perm(pu[2].y, pu[3].y, LO), __builtin_amdgcn_perm(pu[0].y, pu[1].y, LO)};
        *(LAS u32x2*)(tb + 48) = u32x2{__builtin_amdgcn_perm(pu[2].y, pu[3].y, HI), __builtin_amdgcn_perm(pu[0].y, pu[1].y, HI)};
        *(LAS u32x2*)(tb + 64) = u32x2{pk2(kva[3].x, kva[2].x), pk2(kva[1].x, kva[0].x)};
        *(LAS u32x2*)(tb + 80) = u32x2{pk2(kva[3].y, kva[2].y), pk2(kva[1].y, kva[0].y)};
        *(LAS u32x2*)(tb + 96) = u32x2{pk2(kvc[3].x, kvc[2].x), pk2(kvc[1].x, kvc[0].x)};
        *(LAS u32x2*)(tb + 112) = u32x2{pk2(kvc[3].y, kvc[2].y), pk2(kvc[1].y, kvc[0].y)};
#pragma unroll
        for (int i = 0; i < 4; ++i) gs[i] = dpp_add<0x111, 0xf>(gs[i]);
#pragma unroll
        for (int i = 0; i < 4; ++i) gs[i] = dpp_add<0x112, 0xf>(gs[i]);
#pragma unroll
        for (int i = 0; i < 4; ++i) gs[i] = dpp_add<0x114, 0xf>(gs[i]);
#pragma unroll
        for (int i = 0; i < 4; ++i) gs[i] = dpp_add<0x118, 0xf>(gs[i]);
#pragma unroll
        for (int i = 0; i < 4; ++i) gs[i] = dpp_add<0x142, 0xa>(gs[i]);
        if ((lane & 31) == 31) { f32x4 s = {gs[0], gs[1], gs[2], gs[3]}; *(LAS f32x4*)(lds + R_OFF + wave * 32 + ph * 16) = s; }
        STAMP(2);
        asm volatile("s_waitcnt lgkmcnt(0)" ::: "memory");
#pragma unroll
        for (int i = 0; i < 4; ++i) {
            const int c = wave * 32 + i * 8 + (lane >> 3), q = lane & 7;
            const LAS u32x2* s = (const LAS u32x2*)(lds + U_OFF + c * TS + q * 16);
            const u32x2 a = s[0], b = s[1];
            u32x4 d = {a.x, a.y, b.x, b.y};
            __builtin_amdgcn_raw_buffer_store_b128(d, wsr, (int)WS_PAY + c * 32768 + blk * 128 + q * 16, 0, 16);
        }
        STAMP(4);
        __syncthreads();
        STAMP(3);
        if (wave == 7) {
            const LAS float* s = (const LAS float*)(lds + R_OFF) + (lane & 7);
            const float sum = rbf(((s[0] + s[8]) + (s[16] + s[24])) + ((s[32] + s[40]) + (s[48] + s[56])));
            const float g = __builtin_amdgcn_rcpf(1.f + __expf(-sum));
            const u32 gb = pk2(g, 0.f) & 0xffffu;
            u32x4 d;
            d.x = __builtin_amdgcn_readlane(gb, 0) | (__builtin_amdgcn_readlane(gb, 1) << 16);
            d.y = __builtin_amdgcn_readlane(gb, 2) | (__builtin_amdgcn_readlane(gb, 3) << 16);
            d.z = __builtin_amdgcn_readlane(gb, 4) | (__builtin_amdgcn_readlane(gb, 5) << 16);
            d.w = __builtin_amdgcn_readlane(gb, 6) | (__builtin_amdgcn_readlane(gb, 7) << 16);
            if (lane == 0) __builtin_amdgcn_raw_buffer_store_b128(d, wsr, (int)WS_G + blk * 16, 0, 16);
        }
    }
    if (MODE == 1) return;
    {
        const int ch = wave >> 1, ct = wave & 1;
        const LAS char* xb = (const LAS char*)(lds + XY_OFF + ch * XCH);
        const LAS char* a0 = xb + (lane & 15) * XROW + (lane >> 4) * 16;
        const LAS char* b0 = xb + (21 + ct * 16 + (lane & 15)) * XROW + (lane >> 4) * 16;
        f32x4 d0 = {0.f, 0.f, 0.f, 0.f}, d1 = d0;
#pragma unroll
        for (int ks = 0; ks < 8; ++ks) {
            const bf16x8 fa0 = *(const LAS bf16x8*)(a0 + ks * 64), fa1 = *(const LAS bf16x8*)(a0 + 16 * XROW + ks * 64);
            const bf16x8 fb = *(const LAS bf16x8*)(b0 + ks * 64);
            d0 = __builtin_amdgcn_mfma_f32_16x16x32_bf16(fa0, fb, d0, 0, 0, 0);
            d1 = __builtin_amdgcn_mfma_f32_16x16x32_bf16(fa1, fb, d1, 0, 0, 0);
        }
        LAS float* P = (LAS float*)(lds + P_OFF) + ch * PS;
        const int j = ct * 16 + (lane & 15), rg = lane >> 4;
        if (lane == 0 && ct == 0) d0[0] += bf2f(q_dsk0);
        if (rg < 2) {
#pragma unroll
            for (int i = 0; i < 4; ++i) P[4 * rg + i + 8 * j] = d0[i];
        } else if (rg == 2) {
#pragma unroll
            for (int i = 0; i < 4; ++i) P[256 + i + 4 * j] = d0[i];
        } else {
            P[384 + 2 * j] = d0[0]; P[385 + 2 * j] = d0[1]; P[448 + 2 * j] = d0[2]; P[449 + 2 * j] = d0[3];
        }
        if (rg == 0) {
#pragma unroll
            for (int i = 0; i < 4; ++i) P[512 + 32 * i + j] = d1[i];
        } else if (rg == 1 && j == 0) P[640] = d1[0];
    }
    STAMP(7);
    asm volatile("" :: "v"(pf));
    if (MODE == 0) asm volatile("s_waitcnt vmcnt(0)" ::: "memory");
    __syncthreads();
    if (tid < 32) __hip_atomic_store((GAS u64*)(p.ws + WS_FLAG) + tid * 256 + blk, tag, RLX);
    __builtin_amdgcn_sched_barrier(0);
    STAMP(6);
#pragma unroll
    for (int i = 0; i < 2; ++i) {
        const int it = i * 512 + tid, ch = it >> 8, q = it & 255;
        const LAS float* P = (const LAS float*)(lds + P_OFF) + ch * PS;
        float k[9];
        if (q < 32) {
#pragma unroll
            for (int e = 0; e < 9; ++e) k[e] = P[8 * q + e];
        } else if (q < 64) {
            const LAS float* s = P + 256 + 4 * (q - 32);
            const float v0 = s[0], v1 = s[1], v2 = s[2], v3 = s[3], v4 = s[4];
            k[0] = v0; k[1] = v0; k[2] = v1; k[3] = v2; k[4] = v2; k[5] = v2; k[6] = v3; k[7] = v4; k[8] = v4;
        } else if (q < 128) {
            const LAS float* s = P + 384 + 2 * (q - 64);
            const float v0 = s[0], v1 = s[1], v2 = s[2];
            k[0] = v0; k[1] = v0; k[2] = v0; k[3] = v1; k[4] = v1; k[5] = v1; k[6] = v2; k[7] = v2; k[8] = v2;
        } else {
            const LAS float* s = P + 512 + (q - 128);
            const float v0 = s[0], v1 = s[1];
            k[0] = v0; k[1] = v0; k[2] = v0; k[3] = v0; k[4] = (q & 1) ? v1 : v0; k[5] = v1; k[6] = v1; k[7] = v1; k[8] = v1;
        }
        LAS char* e = (LAS char*)(lds + XY_OFF + ch * KV) + 32 + q * 16;
        u32x4 de = {pk2(k[0], k[1]), pk2(k[2], k[3]), pk2(k[4], k[5]), pk2(k[6], k[7])};
        u32x4 dz = {pk2(k[1], k[2]), pk2(k[3], k[4]), pk2(k[5], k[6]), pk2(k[7], k[8])};
        *(LAS u32x4*)e = de; *(LAS u32x4*)(e + KO) = dz;
        if (q == 0) {
            const u32x4 z = {0u, 0u, 0u, 0u};
            u32x4 ha = {0u, 0u, 0u, pk2(0.f, k[0])};
            *(LAS u32x4*)(e - 32) = z; *(LAS u32x4*)(e - 16) = z; *(LAS u32x4*)(e + KO - 32) = z; *(LAS u32x4*)(e + KO - 16) = ha;
        }
    }
    STAMP(8);
#pragma unroll
    for (int i = 0; i < 2; ++i) {
        const int it = i * 512 + tid, ch = it >> 8, q = it & 255;
        LAS char* f = (LAS char*)(lds + XY_OFF + (4 + ch) * KV) + 32 + q * 16;
        if (q == 0) kb[i].x = (kb[i].x & 0xffff0000u) | (pk2(lo2f(kb[i].x) + bf2f(q_sk[i]), 0.f) & 0xffffu);
        u32x4 dy = {__builtin_amdgcn_alignbit(kb[i].y, kb[i].x, 16), __builtin_amdgcn_alignbit(kb[i].z, kb[i].y, 16),
                    __builtin_amdgcn_alignbit(kb[i].w, kb[i].z, 16), __builtin_amdgcn_alignbit(kbn[i], kb[i].w, 16)};
        *(LAS u32x4*)f = kb[i]; *(LAS u32x4*)(f + KO) = dy;
        if (q == 0) {
            const u32x4 z = {0u, 0u, 0u, 0u};
            u32x4 hb = {0u, 0u, 0u, kb[i].x << 16};
            *(LAS u32x4*)(f - 32) = z; *(LAS u32x4*)(f - 16) = z; *(LAS u32x4*)(f + KO - 32) = z; *(LAS u32x4*)(f + KO - 16) = hb;
        }
    }
    if (tid < 240) { const u32x4 z = {0u, 0u, 0u, 0u}; *(LAS u32x4*)(lds + U_OFF + (tid / 30) * US + 4096 + (tid % 30) * 16) = z; }
    u32x2 gq;
    {
        const GAS u64* fl = (const GAS u64*)(p.ws + WS_FLAG) + (blk & 31) * 256 + wave * 32 + (lane & 31);
#pragma unroll 1
        for (u32 spins = 0; spins < (1u << 20); ++spins) {
            const u64 f0 = __hip_atomic_load(fl, RLX);
            if (__all(f0 == tag)) break;
            __builtin_amdgcn_s_sleep(1);
        }
        STAMP(9);
        u32x4 d[4];
#pragma unroll
        for (int i = 0; i < 4; ++i) d[i] = __builtin_amdgcn_raw_buffer_load_b128(wsr, (int)WS_PAY + cg * 32768 + wave * 4096 + (i * 64 + lane) * 16, 0, 16);
        gq = __builtin_amdgcn_raw_buffer_load_b64(wsr, (int)WS_G + tid * 8, 0, 16);
#pragma unroll
        for (int i = 0; i < 4; ++i) {
            const int b = wave * 32 + i * 8 + (lane >> 3), q = lane & 7;
            *(LAS u32x4*)(lds + U_OFF + q * US + 4080 - 16 * b) = d[i];
        }
    }
    __syncthreads();
    if (blk == 0 && tid == 0) __hip_atomic_store((GAS u64*)p.ws, tag, RLX);
    STAMP(10);
    f32x4 acc[8];
#pragma unroll
    for (int c = 0; c < 8; ++c) acc[c] = f32x4{0.f, 0.f, 0.f, 0.f};
    {
        const int j = lane & 15, kg = lane >> 4;
        const LAS char* ab = (const LAS char*)(lds + XY_OFF + wave * KV) + ((j & 1) ? 2 * (1 + j + 8 * kg) : KO + 2 * (j + 8 * kg));
        const LAS char* bb = (const LAS char*)(lds + U_OFF + wave * US) + 2 * (2032 - 16 * j + 8 * kg);
        bf16x8 A[2][8], B[2][8];
        auto ld = [&](int r, bf16x8* a_, bf16x8* b_) {
#pragma unroll
            for (int a = 0; a < 8; ++a) {
                const AQ LAS u32* s = (const AQ LAS u32*)(ab + 512 * a + 64 * r);
                u32x4 t = {s[0], s[1], s[2], s[3]};
                a_[a] = __builtin_bit_cast(bf16x8, t);
                b_[a] = *(const LAS bf16x8*)(bb - 512 * a + 64 * r);
            }
        };
        ld(0, A[0], B[0]);
#pragma unroll
        for (int r = 0; r < 8; ++r) {
#define SP(x) do { if ((x) == 3) __builtin_amdgcn_s_setprio(3); else if ((x) == 2) __builtin_amdgcn_s_setprio(2); else if ((x) == 1) __builtin_amdgcn_s_setprio(1); else __builtin_amdgcn_s_setprio(0); } while (0)
            if (r < 7) ld(r + 1, A[(r + 1) & 1], B[(r + 1) & 1]);
#pragma unroll
            for (int b = 0; b < 8; ++b)
#pragma unroll
                for (int a = 0; a + b < 8; ++a)
                    acc[a + b] = __builtin_amdgcn_mfma_f32_16x16x32_bf16(A[r & 1][a], B[r & 1][b], acc[a + b], 0, 0, 0);
            if (r < 7) {
#pragma unroll
                for (int i = 0; i < 8; ++i) {
                    __builtin_amdgcn_sched_group_barrier(0x008, 2, 0);
                    __builtin_amdgcn_sched_group_barrier(0x100, NDS, 0);
                    __builtin_amdgcn_sched_group_barrier(0x008, 2, 0);
                }
                __builtin_amdgcn_sched_group_barrier(0x008, 4, 0);
            }
            __builtin_amdgcn_sched_barrier(0);
        }
    }
    STAMP(11);
    {
        const int n = lane & 15, kg = lane >> 4;
        const int sg = 4 * n + kg, ps = sg ^ ((sg >> 3) & 7);
        LAS char* rb = (LAS char*)(lds + XY_OFF + wave * KV) + ps * 16;
#pragma unroll
        for (int c = 0; c < 8; ++c) *(LAS f32x4*)(rb + c * 1024) = acc[c];
    }
    __syncthreads();
    STAMP(12);
    {
        const int ps = lane ^ ((lane >> 3) & 7);
        const LAS char* rb = (const LAS char*)(lds + XY_OFF) + wave * 1024 + ps * 16;
        const LAS char* ub = (const LAS char*)(lds + U_OFF) + 2 * (2044 - 4 * tid);
        const f32x2 g01 = {lo2f(gq.x), hi2f(gq.x)}, g23 = {lo2f(gq.y), hi2f(gq.y)};
        f32x2 o01[4], o23[4];
#pragma unroll
        for (int c = 0; c < 4; ++c) {
            const f32x4 a = *(const LAS f32x4*)(rb + c * KV), b = *(const LAS f32x4*)(rb + (4 + c) * KV);
            const u32x2 uu = *(const LAS u32x2*)(ub + c * US);
            const f32x2 u01 = {hi2f(uu.y), lo2f(uu.y)}, u23 = {hi2f(uu.x), lo2f(uu.x)};
            const f32x2 b01 = u01 * f32x2{b.x, b.y}, b23 = u23 * f32x2{b.z, b.w};
            o01[c] = g01 * (f32x2{a.x, a.y} - b01) + b01;
            o23[c] = g23 * (f32x2{a.z, a.w} - b23) + b23;
        }
        GAS char* ob = (GAS char*)(p.out + (size_t)(4 * tid) * DCH + 4 * cg);
        *(GAS u32x2*)(ob) = u32x2{pk2(o01[0].x, o01[1].x), pk2(o01[2].x, o01[3].x)};
        *(GAS u32x2*)(ob + 2 * DCH) = u32x2{pk2(o01[0].y, o01[1].y), pk2(o01[2].y, o01[3].y)};
        *(GAS u32x2*)(ob + 4 * DCH) = u32x2{pk2(o23[0].x, o23[1].x), pk2(o23[2].x, o23[3].x)};
        *(GAS u32x2*)(ob + 6 * DCH) = u32x2{pk2(o23[0].y, o23[1].y), pk2(o23[2].y, o23[3].y)};
    }
    STAMP(13);
}

extern "C" void kernel_launch(void* const* d_in, const int* in_sizes, int n_in, void* d_out, int out_size, void* d_ws,
                              size_t ws_size, hipStream_t stream) {
    Args a;
    a.u = (const u16*)d_in[0]; a.lar = (const u16*)d_in[1]; a.aim = (const u16*)d_in[2]; a.cr = (const u16*)d_in[3];
    a.ci = (const u16*)d_in[4]; a.ldt = (const u16*)d_in[5]; a.dsk = (const u16*)d_in[6]; a.k = (const u16*)d_in[8];
    a.v = (const u16*)d_in[9]; a.shk = (const u16*)d_in[10]; a.ssk = (const u16*)d_in[11]; a.ssd = (const u16*)d_in[12];
    a.gw = (const u16*)d_in[13]; a.out = (u16*)d_out; a.ws = (char*)d_ws;
    (void)hipFuncSetAttribute((const void*)mix_kernel<0>, hipFuncAttributeMaxDynamicSharedMemorySize, LDS_BYTES);
    mix_kernel<0><<<256, 512, LDS_BYTES, stream>>>(a);
}
```

```cpp
#define PFIRST
#define CODE_LINES 126
#include <hip/hip_runtime.h>
#include <stdint.h>

typedef unsigned short u16;
typedef unsigned int u32;
typedef unsigned long long u64;
typedef float f32x4 __attribute__((ext_vector_type(4)));
typedef float f32x2 __attribute__((ext_vector_type(2)));
typedef u32 u32x4 __attribute__((ext_vector_type(4)));
typedef u32 u32x2 __attribute__((ext_vector_type(2)));
typedef __bf16 bf16x8 __attribute__((ext_vector_type(8)));
typedef __bf16 bf16x2 __attribute__((ext_vector_type(2)));
#define GAS __attribute__((address_space(1)))
#define LAS __attribute__((address_space(3)))

constexpr int SEQ = 2048, DCH = 1024;
constexpr int US = 4624;
constexpr int U_OFF = 0;
constexpr int XY_OFF = 8 * US;
constexpr int XROW = 544;
constexpr int XCH = 53 * XROW;
constexpr int P_OFF = XY_OFF + 4 * XCH;
constexpr int PS = 644;
constexpr int R_OFF = P_OFF + 4 * PS * 4;
constexpr int LDS_BYTES = R_OFF + 256;
constexpr int KO = 4160;
constexpr int KV = 8320;
constexpr int Y_OFF = XY_OFF + 8 * KV;
constexpr int YS = 4224, Y2O = 128, Y3O = 2240;
constexpr int G_OFF = Y_OFF + 33920;
constexpr int TS = 136;
constexpr size_t WS_G = 4096, WS_FLAG = 65536, WS_PAY = 131072;

struct Args {
    const u16 *u, *lar, *aim, *cr, *ci, *ldt, *dsk, *k, *v, *shk, *ssk, *ssd, *gw;
    u16* out;
    char* ws;
};

__device__ __forceinline__ float bf2f(u32 h) { return __uint_as_float(h << 16); }
__device__ __forceinline__ float lo2f(u32 d) { return __uint_as_float(d << 16); }
__device__ __forceinline__ float hi2f(u32 d) { return __uint_as_float(d & 0xffff0000u); }
__device__ __forceinline__ u32 pk2(float a, float b) {
    bf16x2 r = {(__bf16)a, (__bf16)b};
    return __builtin_bit_cast(u32, r);
}
__device__ __forceinline__ float rbf(float a) { return lo2f(pk2(a, 0.f)); }
template <int C, int RM>
__device__ __forceinline__ float dpp_add(float v) {
    return v + __int_as_float(__builtin_amdgcn_update_dpp(0, __float_as_int(v), C, RM, 0xf, true));
}
__device__ __forceinline__ float wave_sum63(float v) {
    v = dpp_add<0x111, 0xf>(v); v = dpp_add<0x112, 0xf>(v); v = dpp_add<0x114, 0xf>(v); v = dpp_add<0x118, 0xf>(v);
    v = dpp_add<0x142, 0xa>(v); v = dpp_add<0x143, 0xc>(v);
    return v;
}
__device__ __forceinline__ f32x2 cmul(f32x2 a, f32x2 b) {
    f32x2 r;
    asm("v_pk_mul_f32 %0, %1, %2 op_sel:[0,0] op_sel_hi:[0,1]" : "=v"(r) : "v"(a), "v"(b));
    asm("v_pk_fma_f32 %0, %1, %2, %0 op_sel:[1,1,0] op_sel_hi:[1,0,1] neg_lo:[0,1,0]" : "+v"(r) : "v"(a), "v"(b));
    return r;
}
__device__ __forceinline__ f32x2 cexp(float ar, float ai, float p) {
    float m = __expf(ar * p);
    float rev = (ai * p) * 0.15915494309189535f;
    rev = __builtin_amdgcn_fractf(rev);
    f32x2 r; r.x = m * __builtin_amdgcn_cosf(rev); r.y = m * __builtin_amdgcn_sinf(rev); return r;
}
#define RLX __ATOMIC_RELAXED, __HIP_MEMORY_SCOPE_AGENT
constexpr size_t WS_PROF = 32u << 20;
#define STAMP(i)

#define AQ
#define NDS 3
template <int MODE>
__global__ void __launch_bounds__(512, 2) mix_kernel(Args p) {
    extern __shared__ __attribute__((aligned(16))) char lds[];
    const int tid = threadIdx.x, lane = tid & 63, wave = __builtin_amdgcn_readfirstlane(tid >> 6);
    const int blk = blockIdx.x;
    const int cg = (blk & 7) * 32 + (blk >> 3);
    const __amdgpu_buffer_rsrc_t wsr = __builtin_amdgcn_make_buffer_rsrc((void*)p.ws, 0, 0x7fffffff, 0x00020000);
    STAMP(0);

    const int gch = tid >> 7, gn = tid & 127;
    const int ph = lane >> 5, pc = wave * 32 + (lane & 31);
    u32x2 pu[4], pv[4], pk[7], pg; u32x4 pw0, pw1;
    u32 q_lar, q_aim, q_cr, q_ci, q_ldt;
    { const int o = (4 * cg + gch) * 128 + gn;
      q_lar = p.lar[o]; q_aim = p.aim[o]; q_cr = p.cr[o]; q_ci = p.ci[o]; q_ldt = p.ldt[4 * cg + gch]; }
    __builtin_amdgcn_sched_barrier(0);
    if (MODE != 2) {
        const int t0 = 8 * blk + 4 * ph;
        const GAS u32x2* U = (const GAS u32x2*)p.u; const GAS u32x2* V = (const GAS u32x2*)p.v; const GAS u32x2* K = (const GAS u32x2*)p.k;
#pragma unroll
        for (int i = 0; i < 7; ++i) {
            const int t = t0 - 3 + i;
            pk[i] = K[(t < 0 ? 0 : t) * 256 + pc];
        }
#pragma unroll
        for (int i = 0; i < 4; ++i) { pv[i] = V[(t0 + i) * 256 + pc]; pu[i] = U[(t0 + i) * 256 + pc]; }
        pw0 = ((const GAS u32x4*)p.shk)[2 * pc]; pw1 = ((const GAS u32x4*)p.shk)[2 * pc + 1];
        pg = ((const GAS u32x2*)p.gw)[pc];
    }
    __builtin_amdgcn_sched_barrier(0);
    STAMP(1);
    u32x4 kb[2]; u32 kbn[2]; u32 q_sk[2];
    const u32 q_dsk0 = p.dsk[4 * cg + (wave >> 1)];
    const u64 tag = __hip_atomic_load((GAS u64*)p.ws, RLX) + 1;
    u32 pf = 0;
    {
        u64 pc_; asm volatile("s_getpc_b64 %0" : "=s"(pc_));
        if (tid < CODE_LINES) pf = *(const GAS u32*)((pc_ & ~255ull) + tid * 128);
    }
    __builtin_amdgcn_sched_barrier(0);
    {
        const float dt = rbf(expf(bf2f(q_ldt)));
        const float ar = -rbf(expf(bf2f(q_lar))) * dt, ai = bf2f(q_aim) * dt;
        const f32x2 X0 = {2.f * (bf2f(q_cr) * dt), 2.f * (bf2f(q_ci) * dt)};
        LAS u32* xb = (LAS u32*)(lds + XY_OFF + gch * XCH) + gn;
        constexpr int RW = XROW / 4;
        const f32x2 z = cexp(ar, ai, 1.f), w = cexp(ar, ai, 8.f), w32 = cexp(ar, ai, 256.f), e1024 = cexp(ar, ai, 1024.f);
        f32x2 X[8]; X[0] = X0;
#pragma unroll
        for (int r = 1; r < 8; ++r) X[r] = cmul(X[r - 1], z);
#pragma unroll
        for (int r = 0; r < 8; ++r) xb[r * RW] = pk2(X[r].x, X[r].y);
#pragma unroll
        for (int r = 0; r < 4; ++r) { f32x2 t = cmul(X[2 * r], w32); xb[(8 + r) * RW] = pk2(t.x, t.y); }
        const f32x2 e512 = cmul(w32, w32), e768 = cmul(e512, w32);
        { f32x2 t = cmul(X[0], e512); xb[12 * RW] = pk2(t.x, t.y); t = cmul(X[4], e512); xb[13 * RW] = pk2(t.x, t.y);
          t = cmul(X[0], e768); xb[14 * RW] = pk2(t.x, t.y); t = cmul(X[4], e768); xb[15 * RW] = pk2(t.x, t.y); }
        { const f32x2 x16 = cmul(X[0], e1024); xb[16 * RW] = pk2(x16.x, x16.y);
          f32x2 t = cmul(x16, w32); xb[17 * RW] = pk2(t.x, t.y);
          t = cmul(x16, e512); xb[18 * RW] = pk2(t.x, t.y);
          t = cmul(x16, e768); xb[19 * RW] = pk2(t.x, t.y);
          t = cmul(x16, e1024); xb[20 * RW] = pk2(t.x, t.y); }
        const f32x2 wc = {w.x, -w.y};
        const f32x2 w2 = cmul(wc, wc);
        f32x2 y0 = {1.f, 0.f}, y1 = wc;
#pragma unroll
        for (int j = 0; j < 32; j += 2) {
            xb[(21 + j) * RW] = pk2(y0.x, y0.y); xb[(22 + j) * RW] = pk2(y1.x, y1.y);
            if (j < 30) { y0 = cmul(y0, w2); y1 = cmul(y1, w2); }
        }
    }
    __builtin_amdgcn_sched_barrier(0);
#pragma unroll
    for (int i = 0; i < 2; ++i) {
        const int it = i * 512 + tid, ch = it >> 8, q = it & 255;
        const GAS u32* s = (const GAS u32*)(p.ssk + (4 * cg + ch) * SEQ) + 4 * q;
        kb[i] = *(const GAS u32x4*)s;
        kbn[i] = s[q < 255 ? 4 : 0];
        q_sk[i] = p.ssd[4 * cg + ch];
    }
    __builtin_amdgcn_sched_barrier(0);
    STAMP(5);
    if (MODE != 2) {
        if (blk == 0 && ph == 0) { pk[0] = u32x2{0u, 0u}; pk[1] = pk[0]; pk[2] = pk[0]; }
        f32x2 wa[4], wb[4];
        wa[0] = f32x2{lo2f(pw0.x), lo2f(pw0.z)}; wa[1] = f32x2{hi2f(pw0.x), hi2f(pw0.z)};
        wa[2] = f32x2{lo2f(pw0.y), lo2f(pw0.w)}; wa[3] = f32x2{hi2f(pw0.y), hi2f(pw0.w)};
        wb[0] = f32x2{lo2f(pw1.x), lo2f(pw1.z)}; wb[1] = f32x2{hi2f(pw1.x), hi2f(pw1.z)};
        wb[2] = f32x2{lo2f(pw1.y), lo2f(pw1.w)}; wb[3] = f32x2{hi2f(pw1.y), hi2f(pw1.w)};
        f32x2 ka[7], kc[7], kva[4], kvc[4]; float gs[4];
#pragma unroll
        for (int i = 0; i < 7; ++i) { ka[i] = f32x2{lo2f(pk[i].x), hi2f(pk[i].x)}; kc[i] = f32x2{lo2f(pk[i].y), hi2f(pk[i].y)}; }
        const f32x2 ga = {lo2f(pg.x), hi2f(pg.x)}, gc = {lo2f(pg.y), hi2f(pg.y)};
#pragma unroll
        for (int i = 0; i < 4; ++i) {
            const f32x2 va = {lo2f(pv[i].x), hi2f(pv[i].x)}, vc = {lo2f(pv[i].y), hi2f(pv[i].y)};
            kva[i] = (ka[i] * wa[3] + ka[i + 1] * wa[2] + ka[i + 2] * wa[1] + ka[i + 3] * wa[0]) * va;
            kvc[i] = (kc[i] * wb[3] + kc[i + 1] * wb[2] + kc[i + 2] * wb[1] + kc[i + 3] * wb[0]) * vc;
            const f32x2 ua = {lo2f(pu[i].x), hi2f(pu[i].x)}, uc = {lo2f(pu[i].y), hi2f(pu[i].y)};
            const f32x2 m = ua * ga + uc * gc;
            gs[i] = m.x + m.y;
        }
        LAS char* tb = (LAS char*)(lds + U_OFF) + pc * TS + (1 - ph) * 8;
        constexpr u32 LO = 0x05040100u, HI = 0x07060302u;
        *(LAS u32x2*)(tb + 0) = u32x2{__builtin_amdgcn_perm(pu[2].x, pu[3].x, LO), __builtin_amdgcn_perm(pu[0].x, pu[1].x, LO)};
        *(LAS u32x2*)(tb + 16) = u32x2{__builtin_amdgcn_perm(pu[2].x, pu[3].x, HI), __builtin_amdgcn_perm(pu[0].x, pu[1].x, HI)};
        *(LAS u32x2*)(tb + 32) = u32x2{__builtin_amdgcn_perm(pu[2].y, pu[3].y, LO), __builtin_amdgcn_perm(pu[0].y, pu[1].y, LO)};
        *(LAS u32x2*)(tb + 48) = u32x2{__builtin_amdgcn_perm(pu[2].y, pu[3].y, HI), __builtin_amdgcn_perm(pu[0].y, pu[1].y, HI)};
        *(LAS u32x2*)(tb + 64) = u32x2{pk2(kva[3].x, kva[2].x), pk2(kva[1].x, kva[0].x)};
        *(LAS u32x2*)(tb + 80) = u32x2{pk2(kva[3].y, kva[2].y), pk2(kva[1].y, kva[0].y)};
        *(LAS u32x2*)(tb + 96) = u32x2{pk2(kvc[3].x, kvc[2].x), pk2(kvc[1].x, kvc[0].x)};
        *(LAS u32x2*)(tb + 112) = u32x2{pk2(kvc[3].y, kvc[2].y), pk2(kvc[1].y, kvc[0].y)};
#pragma unroll
        for (int i = 0; i < 4; ++i) gs[i] = dpp_add<0x111, 0xf>(gs[i]);
#pragma unroll
        for (int i = 0; i < 4; ++i) gs[i] = dpp_add<0x112, 0xf>(gs[i]);
#pragma unroll
        for (int i = 0; i < 4; ++i) gs[i] = dpp_add<0x114, 0xf>(gs[i]);
#pragma unroll
        for (int i = 0; i < 4; ++i) gs[i] = dpp_add<0x118, 0xf>(gs[i]);
#pragma unroll
        for (int i = 0; i < 4; ++i) gs[i] = dpp_add<0x142, 0xa>(gs[i]);
        if ((lane & 31) == 31) { f32x4 s = {gs[0], gs[1], gs[2], gs[3]}; *(LAS f32x4*)(lds + R_OFF + wave * 32 + ph * 16) = s; }
        STAMP(2);
        asm volatile("s_waitcnt lgkmcnt(0)" ::: "memory");
#pragma unroll
        for (int i = 0; i < 4; ++i) {
            const int c = wave * 32 + i * 8 + (lane >> 3), q = lane & 7;
            const LAS u32x2* s = (const LAS u32x2*)(lds + U_OFF + c * TS + q * 16);
            const u32x2 a = s[0], b = s[1];
            u32x4 d = {a.x, a.y, b.x, b.y};
            __builtin_amdgcn_raw_buffer_store_b128(d, wsr, (int)WS_PAY + c * 32768 + blk * 128 + q * 16, 0, 16);
        }
        STAMP(4);
        __syncthreads();
        STAMP(3);
        if (wave == 7) {
            const LAS float* s = (const LAS float*)(lds + R_OFF) + (lane & 7);
            const float sum = rbf(((s[0] + s[8]) + (s[16] + s[24])) + ((s[32] + s[40]) + (s[48] + s[56])));
            const float g = __builtin_amdgcn_rcpf(1.f + __expf(-sum));
            const u32 gb = pk2(g, 0.f) & 0xffffu;
            u32x4 d;
            d.x = __builtin_amdgcn_readlane(gb, 0) | (__builtin_amdgcn_readlane(gb, 1) << 16);
            d.y = __builtin_amdgcn_readlane(gb, 2) | (__builtin_amdgcn_readlane(gb, 3) << 16);
            d.z = __builtin_amdgcn_readlane(gb, 4) | (__builtin_amdgcn_readlane(gb, 5) << 16);
            d.w = __builtin_amdgcn_readlane(gb, 6) | (__builtin_amdgcn_readlane(gb, 7) << 16);
            if (lane == 0) __builtin_amdgcn_raw_buffer_store_b128(d, wsr, (int)WS_G + blk * 16, 0, 16);
        }
    }
    if (MODE == 1) return;
    {
        const int ch = wave >> 1, ct = wave & 1;
        const LAS char* xb = (const LAS char*)(lds + XY_OFF + ch * XCH);
        const LAS char* a0 = xb + (lane & 15) * XROW + (lane >> 4) * 16;
        const LAS char* b0 = xb + (21 + ct * 16 + (lane & 15)) * XROW + (lane >> 4) * 16;
        f32x4 d0 = {0.f, 0.f, 0.f, 0.f}, d1 = d0;
#pragma unroll
        for (int ks = 0; ks < 8; ++ks) {
            const bf16x8 fa0 = *(const LAS bf16x8*)(a0 + ks * 64), fa1 = *(const LAS bf16x8*)(a0 + 16 * XROW + ks * 64);
            const bf16x8 fb = *(const LAS bf16x8*)(b0 + ks * 64);
            d0 = __builtin_amdgcn_mfma_f32_16x16x32_bf16(fa0, fb, d0, 0, 0, 0);
            d1 = __builtin_amdgcn_mfma_f32_16x16x32_bf16(fa1, fb, d1, 0, 0, 0);
        }
        LAS float* P = (LAS float*)(lds + P_OFF) + ch * PS;
        const int j = ct * 16 + (lane & 15), rg = lane >> 4;
        if (lane == 0 && ct == 0) d0[0] += bf2f(q_dsk0);
        if (rg < 2) {
#pragma unroll
            for (int i = 0; i < 4; ++i) P[4 * rg + i + 8 * j] = d0[i];
        } else if (rg == 2) {
#pragma unroll
            for (int i = 0; i < 4; ++i) P[256 + i + 4 * j] = d0[i];
        } else {
            P[384 + 2 * j] = d0[0]; P[385 + 2 * j] = d0[1]; P[448 + 2 * j] = d0[2]; P[449 + 2 * j] = d0[3];
        }
        if (rg == 0) {
#pragma unroll
            for (int i = 0; i < 4; ++i) P[512 + 32 * i + j] = d1[i];
        } else if (rg == 1 && j == 0) P[640] = d1[0];
    }
    STAMP(7);
    asm volatile("" :: "v"(pf));
    if (MODE == 0) asm volatile("s_waitcnt vmcnt(0)" ::: "memory");
    __syncthreads();
    if (tid < 32) __hip_atomic_store((GAS u64*)(p.ws + WS_FLAG) + tid * 256 + blk, tag, RLX);
    __builtin_amdgcn_sched_barrier(0);
    STAMP(6);
    auto ycopy = [&](int conv, int q, u32x4 de, u32x4 dz, u32 head) {
        if (q < 128) {
            LAS char* y2 = (LAS char*)(lds + Y_OFF + conv * YS + Y2O) + 16 * q;
            LAS char* y3 = (LAS char*)(lds + Y_OFF + conv * YS + Y3O) + 16 * q;
            *(LAS u32*)(y2 + 28) = de.x; *(LAS u32x2*)(y2 + 32) = u32x2{de.y, de.z}; *(LAS u32*)(y2 + 40) = de.w;
            *(LAS u32*)(y3 + 28) = dz.x; *(LAS u32x2*)(y3 + 32) = u32x2{dz.y, dz.z}; *(LAS u32*)(y3 + 40) = dz.w;
            if (q == 0) {
                const u32x4 z = {0u, 0u, 0u, 0u};
                *(LAS u32x4*)y2 = z; *(LAS u32x2*)(y2 + 16) = u32x2{0u, 0u}; *(LAS u32*)(y2 + 24) = 0u;
                *(LAS u32x4*)y3 = z; *(LAS u32x2*)(y3 + 16) = u32x2{0u, 0u}; *(LAS u32*)(y3 + 24) = head;
            }
        }
    };
#pragma unroll
    for (int i = 0; i < 2; ++i) {
        const int it = i * 512 + tid, ch = it >> 8, q = it & 255;
        const LAS float* P = (const LAS float*)(lds + P_OFF) + ch * PS;
        float k[9];
        if (q < 32) {
#pragma unroll
            for (int e = 0; e < 9; ++e) k[e] = P[8 * q + e];
        } else if (q < 64) {
            const LAS float* s = P + 256 + 4 * (q - 32);
            const float v0 = s[0], v1 = s[1], v2 = s[2], v3 = s[3], v4 = s[4];
            k[0] = v0; k[1] = v0; k[2] = v1; k[3] = v2; k[4] = v2; k[5] = v2; k[6] = v3; k[7] = v4; k[8] = v4;
        } else if (q < 128) {
            const LAS float* s = P + 384 + 2 * (q - 64);
            const float v0 = s[0], v1 = s[1], v2 = s[2];
            k[0] = v0; k[1] = v0; k[2] = v0; k[3] = v1; k[4] = v1; k[5] = v1; k[6] = v2; k[7] = v2; k[8] = v2;
        } else {
            const LAS float* s = P + 512 + (q - 128);
            const float v0 = s[0], v1 = s[1];
            k[0] = v0; k[1] = v0; k[2] = v0; k[3] = v0; k[4] = (q & 1) ? v1 : v0; k[5] = v1; k[6] = v1; k[7] = v1; k[8] = v1;
        }
        LAS char* e = (LAS char*)(lds + XY_OFF + ch * KV) + 32 + q * 16;
        u32x4 de = {pk2(k[0], k[1]), pk2(k[2], k[3]), pk2(k[4], k[5]), pk2(k[6], k[7])};
        u32x4 dz = {pk2(k[1], k[2]), pk2(k[3], k[4]), pk2(k[5], k[6]), pk2(k[7], k[8])};
        *(LAS u32x4*)e = de; *(LAS u32x4*)(e + KO) = dz;
        ycopy(ch, q, de, dz, pk2(0.f, k[0]));
        if (q == 0) {
            const u32x4 z = {0u, 0u, 0u, 0u};
            u32x4 ha = {0u, 0u, 0u, pk2(0.f, k[0])};
            *(LAS u32x4*)(e - 32) = z; *(LAS u32x4*)(e - 16) = z; *(LAS u32x4*)(e + KO - 32) = z; *(LAS u32x4*)(e + KO - 16) = ha;
        }
    }
    STAMP(8);
#pragma unroll
    for (int i = 0; i < 2; ++i) {
        const int it = i * 512 + tid, ch = it >> 8, q = it & 255;
        LAS char* f = (LAS char*)(lds + XY_OFF + (4 + ch) * KV) + 32 + q * 16;
        if (q == 0) kb[i].x = (kb[i].x & 0xffff0000u) | (pk2(lo2f(kb[i].x) + bf2f(q_sk[i]), 0.f) & 0xffffu);
        u32x4 dy = {__builtin_amdgcn_alignbit(kb[i].y, kb[i].x, 16), __builtin_amdgcn_alignbit(kb[i].z, kb[i].y, 16),
                    __builtin_amdgcn_alignbit(kb[i].w, kb[i].z, 16), __builtin_amdgcn_alignbit(kbn[i], kb[i].w, 16)};
        *(LAS u32x4*)f = kb[i]; *(LAS u32x4*)(f + KO) = dy;
        ycopy(4 + ch, q, kb[i], dy, kb[i].x << 16);
        if (q == 0) {
            const u32x4 z = {0u, 0u, 0u, 0u};
            u32x4 hb = {0u, 0u, 0u, kb[i].x << 16};
            *(LAS u32x4*)(f - 32) = z; *(LAS u32x4*)(f - 16) = z; *(LAS u32x4*)(f + KO - 32) = z; *(LAS u32x4*)(f + KO - 16) = hb;
        }
    }
    if (tid < 240) { const u32x4 z = {0u, 0u, 0u, 0u}; *(LAS u32x4*)(lds + U_OFF + (tid / 30) * US + 4096 + (tid % 30) * 16) = z; }
    u32x2 gq;
    {
        const GAS u64* fl = (const GAS u64*)(p.ws + WS_FLAG) + (blk & 31) * 256 + wave * 32 + (lane & 31);
#pragma unroll 1
        for (u32 spins = 0; spins < (1u << 20); ++spins) {
            const u64 f0 = __hip_atomic_load(fl, RLX);
            if (__all(f0 == tag)) break;
            __builtin_amdgcn_s_sleep(1);
        }
        STAMP(9);
        u32x4 d[4];
#pragma unroll
        for (int i = 0; i < 4; ++i) d[i] = __builtin_amdgcn_raw_buffer_load_b128(wsr, (int)WS_PAY + cg * 32768 + wave * 4096 + (i * 64 + lane) * 16, 0, 16);
        gq = __builtin_amdgcn_raw_buffer_load_b64(wsr, (int)WS_G + tid * 8, 0, 16);
#pragma unroll
        for (int i = 0; i < 4; ++i) {
            const int b = wave * 32 + i * 8 + (lane >> 3), q = lane & 7;
            *(LAS u32x4*)(lds + U_OFF + q * US + 4080 - 16 * b) = d[i];
        }
        *(LAS u32x2*)(lds + G_OFF + tid * 8) = gq;
    }
    __syncthreads();
    if (blk == 0 && tid == 0) __hip_atomic_store((GAS u64*)p.ws, tag, RLX);
    STAMP(10);
    constexpr int X1_OFF = Y_OFF + Y2O;
    f32x4 acc[4];
    const int j = lane & 15, kg = lane >> 4;
    const LAS char* ab = (const LAS char*)(lds + XY_OFF + wave * KV) + ((j & 1) ? 2 * (1 + j + 8 * kg) : KO + 2 * (j + 8 * kg));
    const LAS char* bb = (const LAS char*)(lds + U_OFF + wave * US) + 2 * (2032 - 16 * j + 8 * kg);
    const int sgw = 4 * j + kg, psw = sgw ^ ((sgw >> 3) & 7);
    GAS char* const outb = (GAS char*)(p.out + 4 * cg);
    bf16x8 A[2][8], B[2][8];
    auto ld = [&](int r, int n, bf16x8* a_, bf16x8* b_) {
#pragma unroll
        for (int a = 0; a < 8; ++a) if (a < n) {
            const LAS u32* s = (const LAS u32*)(ab + 512 * a + 64 * r);
            u32x4 t = {s[0], s[1], s[2], s[3]};
            a_[a] = __builtin_bit_cast(bf16x8, t);
            b_[a] = *(const LAS bf16x8*)(bb - 512 * a + 64 * r);
        }
    };
#pragma unroll
    for (int c = 0; c < 4; ++c) acc[c] = f32x4{0.f, 0.f, 0.f, 0.f};
    LAS char* a4;
    { const int jm = j & 3;
      const int off = jm == 3 ? XY_OFF + wave * KV + 2 * (1 + j + 8 * kg) : jm == 0 ? XY_OFF + wave * KV + KO + 2 * (j + 8 * kg)
                    : jm == 1 ? Y_OFF + wave * YS + Y2O + 2 * (j - 1 + 8 * kg) : Y_OFF + wave * YS + Y3O + 2 * (j - 2 + 8 * kg);
      a4 = (LAS char*)lds + off; }
    const u32 a4o = (u32)(uintptr_t)a4;
    u32x2 H[2][16];
#define RD8(dst, off) asm volatile("ds_read_b64 %0, %1 offset:%2" : "=v"(dst) : "v"(a4o), "n"(off))
    auto ld2 = [&](int i, u32x2* h_, bf16x8* b_) {
#pragma unroll
        for (int h = 0; h < 2; ++h)
#pragma unroll
            for (int a = 0; a < 4; ++a) b_[4 * h + a] = *(const LAS bf16x8*)(bb - 512 * a + 64 * (2 * i + h));
    };
#define RDA(I, h_) do { \
        RD8(h_[0], 128 * (I)); RD8(h_[1], 128 * (I) + 8); RD8(h_[2], 128 * (I) + 512); RD8(h_[3], 128 * (I) + 520); \
        RD8(h_[4], 128 * (I) + 1024); RD8(h_[5], 128 * (I) + 1032); RD8(h_[6], 128 * (I) + 1536); RD8(h_[7], 128 * (I) + 1544); \
        RD8(h_[8], 128 * (I) + 64); RD8(h_[9], 128 * (I) + 72); RD8(h_[10], 128 * (I) + 576); RD8(h_[11], 128 * (I) + 584); \
        RD8(h_[12], 128 * (I) + 1088); RD8(h_[13], 128 * (I) + 1096); RD8(h_[14], 128 * (I) + 1600); RD8(h_[15], 128 * (I) + 1608); } while (0)
#define WAITA(h_) asm volatile("s_waitcnt lgkmcnt(0)" : "+v"(h_[0]), "+v"(h_[1]), "+v"(h_[2]), "+v"(h_[3]), "+v"(h_[4]), "+v"(h_[5]), "+v"(h_[6]), "+v"(h_[7]), \
        "+v"(h_[8]), "+v"(h_[9]), "+v"(h_[10]), "+v"(h_[11]), "+v"(h_[12]), "+v"(h_[13]), "+v"(h_[14]), "+v"(h_[15]))
    RDA(0, H[0]);
    ld2(0, H[0], B[0]);
    __builtin_amdgcn_sched_barrier(0);
#pragma unroll
    for (int i = 0; i < 4; ++i) {
        WAITA(H[i & 1]);
        __builtin_amdgcn_sched_barrier(0);
        if (i == 0) RDA(1, H[1]); else if (i == 1) RDA(2, H[0]); else if (i == 2) RDA(3, H[1]);
        if (i < 3) ld2(i + 1, H[(i + 1) & 1], B[(i + 1) & 1]); else ld(0, 8, A[0], B[0]);
        constexpr int PA[10] = {3, 2, 2, 1, 1, 1, 0, 0, 0, 0}, PB[10] = {0, 0, 1, 0, 1, 2, 0, 2, 3, 1};
#pragma unroll
        for (int h = 0; h < 2; ++h)
#pragma unroll
            for (int m = 0; m < 10; ++m) {
                const u32x2 lo = H[i & 1][8 * h + 2 * PA[m]], hi = H[i & 1][8 * h + 2 * PA[m] + 1];
                const u32x4 t = {lo.x, lo.y, hi.x, hi.y};
                acc[PA[m] + PB[m]] = __builtin_amdgcn_mfma_f32_16x16x32_bf16(__builtin_bit_cast(bf16x8, t), B[i & 1][4 * h + PB[m]], acc[PA[m] + PB[m]], 0, 0, 0);
            }
        if (i < 3) {
#pragma unroll
            for (int q = 0; q < 8; ++q) { __builtin_amdgcn_sched_group_barrier(0x008, 2, 0); __builtin_amdgcn_sched_group_barrier(0x100, 1, 0); }
            __builtin_amdgcn_sched_group_barrier(0x008, 4, 0);
        } else {
#pragma unroll
            for (int q = 0; q < 8; ++q) {
                __builtin_amdgcn_sched_group_barrier(0x008, 1, 0);
                __builtin_amdgcn_sched_group_barrier(0x100, 3, 0);
                __builtin_amdgcn_sched_group_barrier(0x008, 1, 0);
            }
            __builtin_amdgcn_sched_group_barrier(0x008, 4, 0);
        }
        __builtin_amdgcn_sched_barrier(0);
    }
    {
        LAS char* rb = (LAS char*)(lds + X1_OFF + wave * YS) + psw * 16;
#pragma unroll
        for (int c = 0; c < 4; ++c) *(LAS f32x4*)(rb + c * 1024) = acc[c];
    }
    __syncthreads();
    STAMP(11);
    if (wave < 4) {
        const int ps = lane ^ ((lane >> 3) & 7);
        const LAS char* rb = (const LAS char*)(lds + X1_OFF) + wave * 1024 + ps * 16;
        const LAS char* ub = (const LAS char*)(lds + U_OFF) + 2 * (2044 - 4 * tid);
        const f32x2 g01 = {lo2f(gq.x), hi2f(gq.x)}, g23 = {lo2f(gq.y), hi2f(gq.y)};
        f32x2 o01[4], o23[4];
#pragma unroll
        for (int c = 0; c < 4; ++c) {
            const f32x4 a = *(const LAS f32x4*)(rb + c * YS), b = *(const LAS f32x4*)(rb + (4 + c) * YS);
            const u32x2 uu = *(const LAS u32x2*)(ub + c * US);
            const f32x2 u01 = {hi2f(uu.y), lo2f(uu.y)}, u23 = {hi2f(uu.x), lo2f(uu.x)};
            const f32x2 b01 = u01 * f32x2{b.x, b.y}, b23 = u23 * f32x2{b.z, b.w};
            o01[c] = g01 * (f32x2{a.x, a.y} - b01) + b01;
            o23[c] = g23 * (f32x2{a.z, a.w} - b23) + b23;
        }
        GAS char* ob = outb + (size_t)(4 * tid) * (2 * DCH);
        *(GAS u32x2*)(ob) = u32x2{pk2(o01[0].x, o01[1].x), pk2(o01[2].x, o01[3].x)};
        *(GAS u32x2*)(ob + 2 * DCH) = u32x2{pk2(o01[0].y, o01[1].y), pk2(o01[2].y, o01[3].y)};
        *(GAS u32x2*)(ob + 4 * DCH) = u32x2{pk2(o23[0].x, o23[1].x), pk2(o23[2].x, o23[3].x)};
        *(GAS u32x2*)(ob + 6 * DCH) = u32x2{pk2(o23[0].y, o23[1].y), pk2(o23[2].y, o23[3].y)};
    }
    __builtin_amdgcn_sched_barrier(0);
#pragma unroll
    for (int c = 0; c < 4; ++c) acc[c] = f32x4{0.f, 0.f, 0.f, 0.f};
#pragma unroll
    for (int r = 0; r < 8; ++r) {
        if (r < 7) ld(r + 1, 8, A[(r + 1) & 1], B[(r + 1) & 1]);
        constexpr int QA[26] = {4, 5, 6, 7, 3, 4, 5, 6, 2, 3, 4, 5, 1, 2, 3, 4, 0, 1, 0, 2, 0, 3, 1, 2, 0, 1};
        constexpr int QB[26] = {0, 0, 0, 0, 1, 1, 1, 1, 2, 2, 2, 2, 3, 3, 3, 3, 4, 4, 7, 4, 5, 4, 5, 5, 6, 6};
#pragma unroll
        for (int m = 0; m < 26; ++m)
            acc[QA[m] + QB[m] - 4] = __builtin_amdgcn_mfma_f32_16x16x32_bf16(A[r & 1][QA[m]], B[r & 1][QB[m]], acc[QA[m] + QB[m] - 4], 0, 0, 0);
        if (r < 7) {
#pragma unroll
            for (int i = 0; i < 8; ++i) {
                __builtin_amdgcn_sched_group_barrier(0x008, 2, 0);
                __builtin_amdgcn_sched_group_barrier(0x100, 3, 0);
                __builtin_amdgcn_sched_group_barrier(0x008, 1, 0);
            }
            __builtin_amdgcn_sched_group_barrier(0x008, 2, 0);
        }
        __builtin_amdgcn_sched_barrier(0);
    }
    {
        LAS char* rb = (LAS char*)(lds + XY_OFF + wave * KV) + psw * 16;
#pragma unroll
        for (int c = 0; c < 4; ++c) *(LAS f32x4*)(rb + c * 1024) = acc[c];
    }
    __syncthreads();
    STAMP(12);
    {
        const int sg = (tid & 127) >> 1, ps = sg ^ ((sg >> 3) & 7), t0 = 1024 + 2 * tid;
        const LAS char* rb = (const LAS char*)(lds + XY_OFF) + (wave >> 1) * 1024 + ps * 16 + (tid & 1) * 8;
        const LAS char* ub = (const LAS char*)(lds + U_OFF) + 2 * (2046 - t0);
        const u32 gq2 = *(const LAS u32*)(lds + G_OFF + 2048 + tid * 4);
        const f32x2 g01 = {lo2f(gq2), hi2f(gq2)};
        f32x2 o[4];
#pragma unroll
        for (int c = 0; c < 4; ++c) {
            const f32x2 a = *(const LAS f32x2*)(rb + c * KV), b = *(const LAS f32x2*)(rb + (4 + c) * KV);
            const u32 uu = *(const LAS u32*)(ub + c * US);
            const f32x2 b01 = f32x2{hi2f(uu), lo2f(uu)} * b;
            o[c] = g01 * (a - b01) + b01;
        }
        GAS char* ob = outb + (size_t)t0 * (2 * DCH);
        *(GAS u32x2*)(ob) = u32x2{pk2(o[0].x, o[1].x), pk2(o[2].x, o[3].x)};
        *(GAS u32x2*)(ob + 2 * DCH) = u32x2{pk2(o[0].y, o[1].y), pk2(o[2].y, o[3].y)};
    }
    STAMP(13);
}

extern "C" void kernel_launch(void* const* d_in, const int* in_sizes, int n_in, void* d_out, int out_size, void* d_ws,
                              size_t ws_size, hipStream_t stream) {
    Args a;
    a.u = (const u16*)d_in[0]; a.lar = (const u16*)d_in[1]; a.aim = (const u16*)d_in[2]; a.cr = (const u16*)d_in[3];
    a.ci = (const u16*)d_in[4]; a.ldt = (const u16*)d_in[5]; a.dsk = (const u16*)d_in[6]; a.k = (const u16*)d_in[8];
    a.v = (const u16*)d_in[9]; a.shk = (const u16*)d_in[10]; a.ssk = (const u16*)d_in[11]; a.ssd = (const u16*)d_in[12];
    a.gw = (const u16*)d_in[13]; a.out = (u16*)d_out; a.ws = (char*)d_ws;
    (void)hipFuncSetAttribute((const void*)mix_kernel<0>, hipFuncAttributeMaxDynamicSharedMemorySize, LDS_BYTES);
    mix_kernel<0><<<256, 512, LDS_BYTES, stream>>>(a);
}
```

```cpp
#define PFIRST
#define CODE_LINES 116
#include <hip/hip_runtime.h>
#include <stdint.h>

typedef unsigned short u16;
typedef unsigned int u32;
typedef unsigned long long u64;
typedef float f32x4 __attribute__((ext_vector_type(4)));
typedef float f32x2 __attribute__((ext_vector_type(2)));
typedef u32 u32x4 __attribute__((ext_vector_type(4)));
typedef u32 u32x2 __attribute__((ext_vector_type(2)));
typedef __bf16 bf16x8 __attribute__((ext_vector_type(8)));
typedef __bf16 bf16x2 __attribute__((ext_vector_type(2)));
#define GAS __attribute__((address_space(1)))
#define LAS __attribute__((address_space(3)))

constexpr int SEQ = 2048, DCH = 1024;
constexpr int US = 4624;
constexpr int U_OFF = 0;
constexpr int XY_OFF = 8 * US;
constexpr int XROW = 544;
constexpr int XCH = 53 * XROW;
constexpr int P_OFF = XY_OFF + 4 * XCH;
constexpr int PS = 644;
constexpr int R_OFF = P_OFF + 4 * PS * 4;
constexpr int LDS_BYTES = R_OFF + 256;
constexpr int KO = 4160;
constexpr int KV = 8320;
constexpr int TS = 136;
constexpr size_t WS_G = 4096, WS_FLAG = 65536, WS_PAY = 131072;

struct Args {
    const u16 *u, *lar, *aim, *cr, *ci, *ldt, *dsk, *k, *v, *shk, *ssk, *ssd, *gw;
    u16* out;
    char* ws;
};

__device__ __forceinline__ float bf2f(u32 h) { return __uint_as_float(h << 16); }
__device__ __forceinline__ float lo2f(u32 d) { return __uint_as_float(d << 16); }
__device__ __forceinline__ float hi2f(u32 d) { return __uint_as_float(d & 0xffff0000u); }
__device__ __forceinline__ u32 pk2(float a, float b) {
    bf16x2 r = {(__bf16)a, (__bf16)b};
    return __builtin_bit_cast(u32, r);
}
__device__ __forceinline__ float rbf(float a) { return lo2f(pk2(a, 0.f)); }
template <int C, int RM>
__device__ __forceinline__ float dpp_add(float v) {
    return v + __int_as_float(__builtin_amdgcn_update_dpp(0, __float_as_int(v), C, RM, 0xf, true));
}
__device__ __forceinline__ float wave_sum63(float v) {
    v = dpp_add<0x111, 0xf>(v); v = dpp_add<0x112, 0xf>(v); v = dpp_add<0x114, 0xf>(v); v = dpp_add<0x118, 0xf>(v);
    v = dpp_add<0x142, 0xa>(v); v = dpp_add<0x143, 0xc>(v);
    return v;
}
__device__ __forceinline__ f32x2 cmul(f32x2 a, f32x2 b) {
    f32x2 r;
    asm("v_pk_mul_f32 %0, %1, %2 op_sel:[0,0] op_sel_hi:[0,1]" : "=v"(r) : "v"(a), "v"(b));
    asm("v_pk_fma_f32 %0, %1, %2, %0 op_sel:[1,1,0] op_sel_hi:[1,0,1] neg_lo:[0,1,0]" : "+v"(r) : "v"(a), "v"(b));
    return r;
}
__device__ __forceinline__ f32x2 cexp(float ar, float ai, float p) {
    float m = __expf(ar * p);
    float rev = (ai * p) * 0.15915494309189535f;
    rev = __builtin_amdgcn_fractf(rev);
    f32x2 r; r.x = m * __builtin_amdgcn_cosf(rev); r.y = m * __builtin_amdgcn_sinf(rev); return r;
}
#define RLX __ATOMIC_RELAXED, __HIP_MEMORY_SCOPE_AGENT
constexpr size_t WS_PROF = 32u << 20;
#define STAMP(i)

#define AQ
#define NDS 3
template <int MODE>
__global__ void __launch_bounds__(512, 2) mix_kernel(Args p) {
    extern __shared__ __attribute__((aligned(16))) char lds[];
    const int tid = threadIdx.x, lane = tid & 63, wave = __builtin_amdgcn_readfirstlane(tid >> 6);
    const int blk = blockIdx.x;
    const int cg = (blk & 7) * 32 + (blk >> 3);
    const __amdgpu_buffer_rsrc_t wsr = __builtin_amdgcn_make_buffer_rsrc((void*)p.ws, 0, 0x7fffffff, 0x00020000);
    STAMP(0);

    const int gch = tid >> 7, gn = tid & 127;
    const int ph = lane >> 5, pc = wave * 32 + (lane & 31);
    u32x2 pu[4], pv[4], pk[7], pg; u32x4 pw0, pw1;
    u32 q_lar, q_aim, q_cr, q_ci, q_ldt;
    { const int o = (4 * cg + gch) * 128 + gn;
      q_lar = p.lar[o]; q_aim = p.aim[o]; q_cr = p.cr[o]; q_ci = p.ci[o]; q_ldt = p.ldt[4 * cg + gch]; }
    __builtin_amdgcn_sched_barrier(0);
    if (MODE != 2) {
        const int t0 = 8 * blk + 4 * ph;
        const GAS u32x2* U = (const GAS u32x2*)p.u; const GAS u32x2* V = (const GAS u32x2*)p.v; const GAS u32x2* K = (const GAS u32x2*)p.k;
#pragma unroll
        for (int i = 0; i < 7; ++i) {
            const int t = t0 - 3 + i;
            pk[i] = K[(t < 0 ? 0 : t) * 256 + pc];
        }
#pragma unroll
        for (int i = 0; i < 4; ++i) { pv[i] = V[(t0 + i) * 256 + pc]; pu[i] = U[(t0 + i) * 256 + pc]; }
        pw0 = ((const GAS u32x4*)p.shk)[2 * pc]; pw1 = ((const GAS u32x4*)p.shk)[2 * pc + 1];
        pg = ((const GAS u32x2*)p.gw)[pc];
    }
    __builtin_amdgcn_sched_barrier(0);
    STAMP(1);
    u32x4 kb[2]; u32 kbn[2]; u32 q_sk[2];
    const u32 q_dsk0 = p.dsk[4 * cg + (wave >> 1)];
    const u64 tag = __hip_atomic_load((GAS u64*)p.ws, RLX) + 1;
    u32 pf = 0;
    {
        u64 pc_; asm volatile("s_getpc_b64 %0" : "=s"(pc_));
        if (tid < CODE_LINES) pf = *(const GAS u32*)((pc_ & ~255ull) + tid * 128);
    }
    __builtin_amdgcn_sched_barrier(0);
    {
        const float dt = rbf(expf(bf2f(q_ldt)));
        const float ar = -rbf(expf(bf2f(q_lar))) * dt, ai = bf2f(q_aim) * dt;
        const f32x2 X0 = {2.f * (bf2f(q_cr) * dt), 2.f * (bf2f(q_ci) * dt)};
        LAS u32* xb = (LAS u32*)(lds + XY_OFF + gch * XCH) + gn;
        constexpr int RW = XROW / 4;
        const f32x2 z = cexp(ar, ai, 1.f), w = cexp(ar, ai, 8.f), w32 = cexp(ar, ai, 256.f), e1024 = cexp(ar, ai, 1024.f);
        f32x2 X[8]; X[0] = X0;
#pragma unroll
        for (int r = 1; r < 8; ++r) X[r] = cmul(X[r - 1], z);
#pragma unroll
        for (int r = 0; r < 8; ++r) xb[r * RW] = pk2(X[r].x, X[r].y);
#pragma unroll
        for (int r = 0; r < 4; ++r) { f32x2 t = cmul(X[2 * r], w32); xb[(8 + r) * RW] = pk2(t.x, t.y); }
        const f32x2 e512 = cmul(w32, w32), e768 = cmul(e512, w32);
        { f32x2 t = cmul(X[0], e512); xb[12 * RW] = pk2(t.x, t.y); t = cmul(X[4], e512); xb[13 * RW] = pk2(t.x, t.y);
          t = cmul(X[0], e768); xb[14 * RW] = pk2(t.x, t.y); t = cmul(X[4], e768); xb[15 * RW] = pk2(t.x, t.y); }
        { const f32x2 x16 = cmul(X[0], e1024); xb[16 * RW] = pk2(x16.x, x16.y);
          f32x2 t = cmul(x16, w32); xb[17 * RW] = pk2(t.x, t.y);
          t = cmul(x16, e512); xb[18 * RW] = pk2(t.x, t.y);
          t = cmul(x16, e768); xb[19 * RW] = pk2(t.x, t.y);
          t = cmul(x16, e1024); xb[20 * RW] = pk2(t.x, t.y); }
        const f32x2 wc = {w.x, -w.y};
        const f32x2 w2 = cmul(wc, wc);
        f32x2 y0 = {1.f, 0.f}, y1 = wc;
#pragma unroll
        for (int j = 0; j < 32; j += 2) {
            xb[(21 + j) * RW] = pk2(y0.x, y0.y); xb[(22 + j) * RW] = pk2(y1.x, y1.y);
            if (j < 30) { y0 = cmul(y0, w2); y1 = cmul(y1, w2); }
        }
    }
    __builtin_amdgcn_sched_barrier(0);
#pragma unroll
    for (int i = 0; i < 2; ++i) {
        const int it = i * 512 + tid, ch = it >> 8, q = it & 255;
        const GAS u32* s = (const GAS u32*)(p.ssk + (4 * cg + ch) * SEQ) + 4 * q;
        kb[i] = *(const GAS u32x4*)s;
        kbn[i] = s[q < 255 ? 4 : 0];
        q_sk[i] = p.ssd[4 * cg + ch];
    }
    __builtin_amdgcn_sched_barrier(0);
    STAMP(5);
    if (MODE != 2) {
        if (blk == 0 && ph == 0) { pk[0] = u32x2{0u, 0u}; pk[1] = pk[0]; pk[2] = pk[0]; }
        f32x2 wa[4], wb[4];
        wa[0] = f32x2{lo2f(pw0.x), lo2f(pw0.z)}; wa[1] = f32x2{hi2f(pw0.x), hi2f(pw0.z)};
        wa[2] = f32x2{lo2f(pw0.y), lo2f(pw0.w)}; wa[3] = f32x2{hi2f(pw0.y), hi2f(pw0.w)};
        wb[0] = f32x2{lo2f(pw1.x), lo2f(pw1.z)}; wb[1] = f32x2{hi2f(pw1.x), hi2f(pw1.z)};
        wb[2] = f32x2{lo2f(pw1.y), lo2f(pw1.w)}; wb[3] = f32x2{hi2f(pw1.y), hi2f(pw1.w)};
        f32x2 ka[7], kc[7], kva[4], kvc[4]; float gs[4];
#pragma unroll
        for (int i = 0; i < 7; ++i) { ka[i] = f32x2{lo2f(pk[i].x), hi2f(pk[i].x)}; kc[i] = f32x2{lo2f(pk[i].y), hi2f(pk[i].y)}; }
        const f32x2 ga = {lo2f(pg.x), hi2f(pg.x)}, gc = {lo2f(pg.y), hi2f(pg.y)};
#pragma unroll
        for (int i = 0; i < 4; ++i) {
            const f32x2 va = {lo2f(pv[i].x), hi2f(pv[i].x)}, vc = {lo2f(pv[i].y), hi2f(pv[i].y)};
            kva[i] = (ka[i] * wa[3] + ka[i + 1] * wa[2] + ka[i + 2] * wa[1] + ka[i + 3] * wa[0]) * va;
            kvc[i] = (kc[i] * wb[3] + kc[i + 1] * wb[2] + kc[i + 2] * wb[1] + kc[i + 3] * wb[0]) * vc;
            const f32x2 ua = {lo2f(pu[i].x), hi2f(pu[i].x)}, uc = {lo2f(pu[i].y), hi2f(pu[i].y)};
            const f32x2 m = ua * ga + uc * gc;
            gs[i] = m.x + m.y;
        }
        LAS char* tb = (LAS char*)(lds + U_OFF) + pc * TS + (1 - ph) * 8;
        constexpr u32 LO = 0x05040100u, HI = 0x07060302u;
        *(LAS u32x2*)(tb + 0) = u32x2{__builtin_amdgcn_perm(pu[2].x, pu[3].x, LO), __builtin_amdgcn_perm(pu[0].x, pu[1].x, LO)};
        *(LAS u32x2*)(tb + 16) = u32x2{__builtin_amdgcn_perm(pu[2].x, pu[3].x, HI), __builtin_amdgcn_perm(pu[0].x, pu[1].x, HI)};
        *(LAS u32x2*)(tb + 32) = u32x2{__builtin_amdgcn_perm(pu[2].y, pu[3].y, LO), __builtin_amdgcn_perm(pu[0].y, pu[1].y, LO)};
        *(LAS u32x2*)(tb + 48) = u32x2{__builtin_amdgcn_perm(pu[2].y, pu[3].y, HI), __builtin_amdgcn_perm(pu[0].y, pu[1].y, HI)};
        *(LAS u32x2*)(tb + 64) = u32x2{pk2(kva[3].x, kva[2].x), pk2(kva[1].x, kva[0].x)};
        *(LAS u32x2*)(tb + 80) = u32x2{pk2(kva[3].y, kva[2].y), pk2(kva[1].y, kva[0].y)};
        *(LAS u32x2*)(tb + 96) = u32x2{pk2(kvc[3].x, kvc[2].x), pk2(kvc[1].x, kvc[0].x)};
        *(LAS u32x2*)(tb + 112) = u32x2{pk2(kvc[3].y, kvc[2].y), pk2(kvc[1].y, kvc[0].y)};
#pragma unroll
        for (int i = 0; i < 4; ++i) gs[i] = dpp_add<0x111, 0xf>(gs[i]);
#pragma unroll
        for (int i = 0; i < 4; ++i) gs[i] = dpp_add<0x112, 0xf>(gs[i]);
#pragma unroll
        for (int i = 0; i < 4; ++i) gs[i] = dpp_add<0x114, 0xf>(gs[i]);
#pragma unroll
        for (int i = 0; i < 4; ++i) gs[i] = dpp_add<0x118, 0xf>(gs[i]);
#pragma unroll
        for (int i = 0; i < 4; ++i) gs[i] = dpp_add<0x142, 0xa>(gs[i]);
        if ((lane & 31) == 31) { f32x4 s = {gs[0], gs[1], gs[2], gs[3]}; *(LAS f32x4*)(lds + R_OFF + wave * 32 + ph * 16) = s; }
        STAMP(2);
        asm volatile("s_waitcnt lgkmcnt(0)" ::: "memory");
#pragma unroll
        for (int i = 0; i < 4; ++i) {
            const int c = wave * 32 + i * 8 + (lane >> 3), q = lane & 7;
            const LAS u32x2* s = (const LAS u32x2*)(lds + U_OFF + c * TS + q * 16);
            const u32x2 a = s[0], b = s[1];
            u32x4 d = {a.x, a.y, b.x, b.y};
            __builtin_amdgcn_raw_buffer_store_b128(d, wsr, (int)WS_PAY + c * 32768 + blk * 128 + q * 16, 0, 16);
        }
        STAMP(4);
        __syncthreads();
        STAMP(3);
        if (wave == 7) {
            const LAS float* s = (const LAS float*)(lds + R_OFF) + (lane & 7);
            const float sum = rbf(((s[0] + s[8]) + (s[16] + s[24])) + ((s[32] + s[40]) + (s[48] + s[56])));
            const float g = __builtin_amdgcn_rcpf(1.f + __expf(-sum));
            const u32 gb = pk2(g, 0.f) & 0xffffu;
            u32x4 d;
            d.x = __builtin_amdgcn_readlane(gb, 0) | (__builtin_amdgcn_readlane(gb, 1) << 16);
            d.y = __builtin_amdgcn_readlane(gb, 2) | (__builtin_amdgcn_readlane(gb, 3) << 16);
            d.z = __builtin_amdgcn_readlane(gb, 4) | (__builtin_amdgcn_readlane(gb, 5) << 16);
            d.w = __builtin_amdgcn_readlane(gb, 6) | (__builtin_amdgcn_readlane(gb, 7) << 16);
            if (lane == 0) __builtin_amdgcn_raw_buffer_store_b128(d, wsr, (int)WS_G + blk * 16, 0, 16);
        }
    }
    if (MODE == 1) return;
    {
        const int ch = wave >> 1, ct = wave & 1;
        const LAS char* xb = (const LAS char*)(lds + XY_OFF + ch * XCH);
        const LAS char* a0 = xb + (lane & 15) * XROW + (lane >> 4) * 16;
        const LAS char* b0 = xb + (21 + ct * 16 + (lane & 15)) * XROW + (lane >> 4) * 16;
        f32x4 d0 = {0.f, 0.f, 0.f, 0.f}, d1 = d0;
#pragma unroll
        for (int ks = 0; ks < 8; ++ks) {
            const bf16x8 fa0 = *(const LAS bf16x8*)(a0 + ks * 64), fa1 = *(const LAS bf16x8*)(a0 + 16 * XROW + ks * 64);
            const bf16x8 fb = *(const LAS bf16x8*)(b0 + ks * 64);
            d0 = __builtin_amdgcn_mfma_f32_16x16x32_bf16(fa0, fb, d0, 0, 0, 0);
            d1 = __builtin_amdgcn_mfma_f32_16x16x32_bf16(fa1, fb, d1, 0, 0, 0);
        }
        LAS float* P = (LAS float*)(lds + P_OFF) + ch * PS;
        const int j = ct * 16 + (lane & 15), rg = lane >> 4;
        if (lane == 0 && ct == 0) d0[0] += bf2f(q_dsk0);
        if (rg < 2) {
#pragma unroll
            for (int i = 0; i < 4; ++i) P[4 * rg + i + 8 * j] = d0[i];
        } else if (rg == 2) {
#pragma unroll
            for (int i = 0; i < 4; ++i) P[256 + i + 4 * j] = d0[i];
        } else {
            P[384 + 2 * j] = d0[0]; P[385 + 2 * j] = d0[1]; P[448 + 2 * j] = d0[2]; P[449 + 2 * j] = d0[3];
        }
        if (rg == 0) {
#pragma unroll
            for (int i = 0; i < 4; ++i) P[512 + 32 * i + j] = d1[i];
        } else if (rg == 1 && j == 0) P[640] = d1[0];
    }
    STAMP(7);
    asm volatile("" :: "v"(pf));
    if (MODE == 0) asm volatile("s_waitcnt vmcnt(0)" ::: "memory");
    __syncthreads();
    if (tid < 32) __hip_atomic_store((GAS u64*)(p.ws + WS_FLAG) + tid * 256 + blk, tag, RLX);
    __builtin_amdgcn_sched_barrier(0);
    STAMP(6);
#pragma unroll
    for (int i = 0; i < 2; ++i) {
        const int it = i * 512 + tid, ch = it >> 8, q = it & 255;
        const LAS float* P = (const LAS float*)(lds + P_OFF) + ch * PS;
        float k[9];
        if (q < 32) {
#pragma unroll
            for (int e = 0; e < 9; ++e) k[e] = P[8 * q + e];
        } else if (q < 64) {
            const LAS float* s = P + 256 + 4 * (q - 32);
            const float v0 = s[0], v1 = s[1], v2 = s[2], v3 = s[3], v4 = s[4];
            k[0] = v0; k[1] = v0; k[2] = v1; k[3] = v2; k[4] = v2; k[5] = v2; k[6] = v3; k[7] = v4; k[8] = v4;
        } else if (q < 128) {
            const LAS float* s = P + 384 + 2 * (q - 64);
            const float v0 = s[0], v1 = s[1], v2 = s[2];
            k[0] = v0; k[1] = v0; k[2] = v0; k[3] = v1; k[4] = v1; k[5] = v1; k[6] = v2; k[7] = v2; k[8] = v2;
        } else {
            const LAS float* s = P + 512 + (q - 128);
            const float v0 = s[0], v1 = s[1];
            k[0] = v0; k[1] = v0; k[2] = v0; k[3] = v0; k[4] = (q & 1) ? v1 : v0; k[5] = v1; k[6] = v1; k[7] = v1; k[8] = v1;
        }
        LAS char* e = (LAS char*)(lds + XY_OFF + ch * KV) + 32 + q * 16;
        u32x4 de = {pk2(k[0], k[1]), pk2(k[2], k[3]), pk2(k[4], k[5]), pk2(k[6], k[7])};
        u32x4 dz = {pk2(k[1], k[2]), pk2(k[3], k[4]), pk2(k[5], k[6]), pk2(k[7], k[8])};
        *(LAS u32x4*)e = de; *(LAS u32x4*)(e + KO) = dz;
        if (q == 0) {
            const u32x4 z = {0u, 0u, 0u, 0u};
            u32x4 ha = {0u, 0u, 0u, pk2(0.f, k[0])};
            *(LAS u32x4*)(e - 32) = z; *(LAS u32x4*)(e - 16) = z; *(LAS u32x4*)(e + KO - 32) = z; *(LAS u32x4*)(e + KO - 16) = ha;
        }
    }
    STAMP(8);
#pragma unroll
    for (int i = 0; i < 2; ++i) {
        const int it = i * 512 + tid, ch = it >> 8, q = it & 255;
        LAS char* f = (LAS char*)(lds + XY_OFF + (4 + ch) * KV) + 32 + q * 16;
        if (q == 0) kb[i].x = (kb[i].x & 0xffff0000u) | (pk2(lo2f(kb[i].x) + bf2f(q_sk[i]), 0.f) & 0xffffu);
        u32x4 dy = {__builtin_amdgcn_alignbit(kb[i].y, kb[i].x, 16), __builtin_amdgcn_alignbit(kb[i].z, kb[i].y, 16),
                    __builtin_amdgcn_alignbit(kb[i].w, kb[i].z, 16), __builtin_amdgcn_alignbit(kbn[i], kb[i].w, 16)};
        *(LAS u32x4*)f = kb[i]; *(LAS u32x4*)(f + KO) = dy;
        if (q == 0) {
            const u32x4 z = {0u, 0u, 0u, 0u};
            u32x4 hb = {0u, 0u, 0u, kb[i].x << 16};
            *(LAS u32x4*)(f - 32) = z; *(LAS u32x4*)(f - 16) = z; *(LAS u32x4*)(f + KO - 32) = z; *(LAS u32x4*)(f + KO - 16) = hb;
        }
    }
    if (tid < 240) { const u32x4 z = {0u, 0u, 0u, 0u}; *(LAS u32x4*)(lds + U_OFF + (tid / 30) * US + 4096 + (tid % 30) * 16) = z; }
    u32x2 gq;
    constexpr int G_OFF = XY_OFF + 8 * KV + 32768;
    {
        const GAS u64* fl = (const GAS u64*)(p.ws + WS_FLAG) + (blk & 31) * 256 + wave * 32 + (lane & 31);
#pragma unroll 1
        for (u32 spins = 0; spins < (1u << 20); ++spins) {
            const u64 f0 = __hip_atomic_load(fl, RLX);
            if (__all(f0 == tag)) break;
            __builtin_amdgcn_s_sleep(1);
        }
        STAMP(9);
        u32x4 d[4];
#pragma unroll
        for (int i = 0; i < 4; ++i) d[i] = __builtin_amdgcn_raw_buffer_load_b128(wsr, (int)WS_PAY + cg * 32768 + wave * 4096 + (i * 64 + lane) * 16, 0, 16);
        gq = __builtin_amdgcn_raw_buffer_load_b64(wsr, (int)WS_G + tid * 8, 0, 16);
#pragma unroll
        for (int i = 0; i < 4; ++i) {
            const int b = wave * 32 + i * 8 + (lane >> 3), q = lane & 7;
            *(LAS u32x4*)(lds + U_OFF + q * US + 4080 - 16 * b) = d[i];
        }
        *(LAS u32x2*)(lds + G_OFF + tid * 8) = gq;
    }
    __syncthreads();
    if (blk == 0 && tid == 0) __hip_atomic_store((GAS u64*)p.ws, tag, RLX);
    STAMP(10);
    constexpr int X1_OFF = XY_OFF + 8 * KV;
    f32x4 acc[4];
    const int j = lane & 15, kg = lane >> 4;
    const LAS char* ab = (const LAS char*)(lds + XY_OFF + wave * KV) + ((j & 1) ? 2 * (1 + j + 8 * kg) : KO + 2 * (j + 8 * kg));
    const LAS char* bb = (const LAS char*)(lds + U_OFF + wave * US) + 2 * (2032 - 16 * j + 8 * kg);
    const int sgw = 4 * j + kg, psw = sgw ^ ((sgw >> 3) & 7);
    GAS char* const outb = (GAS char*)(p.out + 4 * cg);
    bf16x8 A[2][8], B[2][8];
    auto ld = [&](int r, int n, bf16x8* a_, bf16x8* b_) {
#pragma unroll
        for (int a = 0; a < 8; ++a) if (a < n) {
            const LAS u32* s = (const LAS u32*)(ab + 512 * a + 64 * r);
            u32x4 t = {s[0], s[1], s[2], s[3]};
            a_[a] = __builtin_bit_cast(bf16x8, t);
            b_[a] = *(const LAS bf16x8*)(bb - 512 * a + 64 * r);
        }
    };
#pragma unroll
    for (int c = 0; c < 4; ++c) acc[c] = f32x4{0.f, 0.f, 0.f, 0.f};
    auto ld2 = [&](int i, bf16x8* a_, bf16x8* b_) {
#pragma unroll
        for (int h = 0; h < 2; ++h)
#pragma unroll
            for (int a = 0; a < 4; ++a) {
                const LAS u32* s = (const LAS u32*)(ab + 512 * a + 64 * (2 * i + h));
                u32x4 t = {s[0], s[1], s[2], s[3]};
                a_[4 * h + a] = __builtin_bit_cast(bf16x8, t);
                b_[4 * h + a] = *(const LAS bf16x8*)(bb - 512 * a + 64 * (2 * i + h));
            }
    };
    ld2(0, A[0], B[0]);
#pragma unroll
    for (int i = 0; i < 4; ++i) {
        if (i < 3) ld2(i + 1, A[(i + 1) & 1], B[(i + 1) & 1]); else ld(0, 8, A[0], B[0]);
        constexpr int PA[10] = {3, 2, 2, 1, 1, 1, 0, 0, 0, 0}, PB[10] = {0, 0, 1, 0, 1, 2, 0, 2, 3, 1};
#pragma unroll
        for (int h = 0; h < 2; ++h)
#pragma unroll
            for (int m = 0; m < 10; ++m)
                acc[PA[m] + PB[m]] = __builtin_amdgcn_mfma_f32_16x16x32_bf16(A[i & 1][4 * h + PA[m]], B[i & 1][4 * h + PB[m]], acc[PA[m] + PB[m]], 0, 0, 0);
#pragma unroll
        for (int q = 0; q < 8; ++q) {
            __builtin_amdgcn_sched_group_barrier(0x008, 1, 0);
            __builtin_amdgcn_sched_group_barrier(0x100, 3, 0);
            __builtin_amdgcn_sched_group_barrier(0x008, 1, 0);
        }
        __builtin_amdgcn_sched_group_barrier(0x008, 4, 0);
        __builtin_amdgcn_sched_barrier(0);
    }
    {
        LAS char* rb = (LAS char*)(lds + X1_OFF + wave * 4096) + psw * 16;
#pragma unroll
        for (int c = 0; c < 4; ++c) *(LAS f32x4*)(rb + c * 1024) = acc[c];
    }
    __syncthreads();
    STAMP(11);
    if (wave < 4) {
        const int ps = lane ^ ((lane >> 3) & 7);
        const LAS char* rb = (const LAS char*)(lds + X1_OFF) + wave * 1024 + ps * 16;
        const LAS char* ub = (const LAS char*)(lds + U_OFF) + 2 * (2044 - 4 * tid);
        const f32x2 g01 = {lo2f(gq.x), hi2f(gq.x)}, g23 = {lo2f(gq.y), hi2f(gq.y)};
        f32x2 o01[4], o23[4];
#pragma unroll
        for (int c = 0; c < 4; ++c) {
            const f32x4 a = *(const LAS f32x4*)(rb + c * 4096), b = *(const LAS f32x4*)(rb + (4 + c) * 4096);
            const u32x2 uu = *(const LAS u32x2*)(ub + c * US);
            const f32x2 u01 = {hi2f(uu.y), lo2f(uu.y)}, u23 = {hi2f(uu.x), lo2f(uu.x)};
            const f32x2 b01 = u01 * f32x2{b.x, b.y}, b23 = u23 * f32x2{b.z, b.w};
            o01[c] = g01 * (f32x2{a.x, a.y} - b01) + b01;
            o23[c] = g23 * (f32x2{a.z, a.w} - b23) + b23;
        }
        GAS char* ob = outb + (size_t)(4 * tid) * (2 * DCH);
        *(GAS u32x2*)(ob) = u32x2{pk2(o01[0].x, o01[1].x), pk2(o01[2].x, o01[3].x)};
        *(GAS u32x2*)(ob + 2 * DCH) = u32x2{pk2(o01[0].y, o01[1].y), pk2(o01[2].y, o01[3].y)};
        *(GAS u32x2*)(ob + 4 * DCH) = u32x2{pk2(o23[0].x, o23[1].x), pk2(o23[2].x, o23[3].x)};
        *(GAS u32x2*)(ob + 6 * DCH) = u32x2{pk2(o23[0].y, o23[1].y), pk2(o23[2].y, o23[3].y)};
    }
    __builtin_amdgcn_sched_barrier(0);
#pragma unroll
    for (int c = 0; c < 4; ++c) acc[c] = f32x4{0.f, 0.f, 0.f, 0.f};
#pragma unroll
    for (int r = 0; r < 8; ++r) {
        if (r < 7) ld(r + 1, 8, A[(r + 1) & 1], B[(r + 1) & 1]);
        constexpr int QA[26] = {4, 5, 6, 7, 3, 4, 5, 6, 2, 3, 4, 5, 1, 2, 3, 4, 0, 1, 0, 2, 0, 3, 1, 2, 0, 1};
        constexpr int QB[26] = {0, 0, 0, 0, 1, 1, 1, 1, 2, 2, 2, 2, 3, 3, 3, 3, 4, 4, 7, 4, 5, 4, 5, 5, 6, 6};
#pragma unroll
        for (int m = 0; m < 26; ++m)
            acc[QA[m] + QB[m] - 4] = __builtin_amdgcn_mfma_f32_16x16x32_bf16(A[r & 1][QA[m]], B[r & 1][QB[m]], acc[QA[m] + QB[m] - 4], 0, 0, 0);
        if (r < 7) {
#pragma unroll
            for (int i = 0; i < 8; ++i) {
                __builtin_amdgcn_sched_group_barrier(0x008, 2, 0);
                __builtin_amdgcn_sched_group_barrier(0x100, 3, 0);
                __builtin_amdgcn_sched_group_barrier(0x008, 1, 0);
            }
            __builtin_amdgcn_sched_group_barrier(0x008, 2, 0);
        }
        __builtin_amdgcn_sched_barrier(0);
    }
    {
        LAS char* rb = (LAS char*)(lds + XY_OFF + wave * KV) + psw * 16;
#pragma unroll
        for (int c = 0; c < 4; ++c) *(LAS f32x4*)(rb + c * 1024) = acc[c];
    }
    __syncthreads();
    STAMP(12);
    {
        const int sg = (tid & 127) >> 1, ps = sg ^ ((sg >> 3) & 7), t0 = 1024 + 2 * tid;
        const LAS char* rb = (const LAS char*)(lds + XY_OFF) + (wave >> 1) * 1024 + ps * 16 + (tid & 1) * 8;
        const LAS char* ub = (const LAS char*)(lds + U_OFF) + 2 * (2046 - t0);
        const u32 gq2 = *(const LAS u32*)(lds + G_OFF + 2048 + tid * 4);
        const f32x2 g01 = {lo2f(gq2), hi2f(gq2)};
        f32x2 o[4];
#pragma unroll
        for (int c = 0; c < 4; ++c) {
            const f32x2 a = *(const LAS f32x2*)(rb + c * KV), b = *(const LAS f32x2*)(rb + (4 + c) * KV);
            const u32 uu = *(const LAS u32*)(ub + c * US);
            const f32x2 b01 = f32x2{hi2f(uu), lo2f(uu)} * b;
            o[c] = g01 * (a - b01) + b01;
        }
        GAS char* ob = outb + (size_t)t0 * (2 * DCH);
        *(GAS u32x2*)(ob) = u32x2{pk2(o[0].x, o[1].x), pk2(o[2].x, o[3].x)};
        *(GAS u32x2*)(ob + 2 * DCH) = u32x2{pk2(o[0].y, o[1].y), pk2(o[2].y, o[3].y)};
    }
    STAMP(13);
}

extern "C" void kernel_launch(void* const* d_in, const int* in_sizes, int n_in, void* d_out, int out_size, void* d_ws,
                              size_t ws_size, hipStream_t stream) {
    Args a;
    a.u = (const u16*)d_in[0]; a.lar = (const u16*)d_in[1]; a.aim = (const u16*)d_in[2]; a.cr = (const u16*)d_in[3];
    a.ci = (const u16*)d_in[4]; a.ldt = (const u16*)d_in[5]; a.dsk = (const u16*)d_in[6]; a.k = (const u16*)d_in[8];
    a.v = (const u16*)d_in[9]; a.shk = (const u16*)d_in[10]; a.ssk = (const u16*)d_in[11]; a.ssd = (const u16*)d_in[12];
    a.gw = (const u16*)d_in[13]; a.out = (u16*)d_out; a.ws = (char*)d_ws;
    (void)hipFuncSetAttribute((const void*)mix_kernel<0>, hipFuncAttributeMaxDynamicSharedMemorySize, LDS_BYTES);
    mix_kernel<0><<<256, 512, LDS_BYTES, stream>>>(a);
}
```
